```python
import torch

def _softmax(x, axis=-1):
    return torch.softmax(x, dim=axis)
def _gelu(x):
    return torch.nn.functional.gelu(x, approximate="tanh")
def _silu(x):
    return torch.nn.functional.silu(x)
def _sigmoid(x):
    return torch.sigmoid(x)
def _layernorm(x, axis=-1, eps=1e-5):
    m = x.mean(dim=axis, keepdim=True)
    v = x.var(dim=axis, keepdim=True, unbiased=False)
    return (x - m) / torch.sqrt(v + eps)
def _scatter_add(tgt, idx, src, axis=0):
    return tgt.clone().index_add_(axis, idx, src)
def _segment_sum(src, seg, n):
    out = torch.zeros((n,) + src.shape[1:], dtype=src.dtype)
    return out.index_add_(0, seg, src)
def _pad(x, pw):
    flat = tuple(v for pair in reversed(pw) for v in pair)
    return torch.nn.functional.pad(x, flat)

kernel_name = 'gated_mix__sliding_local_dilated__linear_kernel_hedgehog__prompt'

def setup_inputs():

    _w = (lambda a: (lambda t: t.to(torch.bfloat16) if t.is_floating_point() else t)(torch.as_tensor(a)))
    def _A_setup():
        B, S, D = 1, 2048, 448
        return {
            "x": _w((lambda *s: torch.randn(*s, dtype=torch.bfloat16) * 0.05)(B, S, D)),
            "wqkv": _w((lambda *s: torch.randn(*s, dtype=torch.bfloat16) * 0.8)(D, 3 * D)),
            "wo": _w((lambda *s: torch.randn(*s, dtype=torch.bfloat16) * 0.05)(D, D)),
        }
    _A_in = _A_setup()

    def _B_setup():
        B, S, D, H = 1, 2048, 448, 16
        Dh = D // H
        return {
            "x": _w((lambda *s: torch.randn(*s, dtype=torch.bfloat16) * 0.05)(B, S, D)),
            "wqkv": _w((lambda *s: torch.randn(*s, dtype=torch.bfloat16) * 1.6)(D, 3 * D)),
            "w_phi": _w((lambda *s: torch.randn(*s, dtype=torch.bfloat16) * 1.6)(H, Dh, Dh) * (Dh ** -0.5)),
            "wo": _w((lambda *s: torch.randn(*s, dtype=torch.bfloat16) * 0.2)(D, D)),
        }
    _B_in = _B_setup()

    return {
        "A_x": _A_in["x"],
        "A_wqkv": _A_in["wqkv"],
        "A_wo": _A_in["wo"],
        "B_x": _B_in["x"],
        "B_wqkv": _B_in["wqkv"],
        "B_w_phi": _B_in["w_phi"],
        "B_wo": _B_in["wo"],
        "gate_w": _w((lambda *s: torch.randn(*s, dtype=torch.bfloat16) * 0.05)(448) * 16.0),
    }

def reference(A_x, A_wqkv, A_wo, B_x, B_wqkv, B_w_phi, B_wo, gate_w):
    def _A_ref(x, wqkv, wo):
        B, S, D = x.shape
        H = 16; Dh = D // H
        W, dil = 8, 2
        qkv = (lambda x, *d: x.permute(*d))((x @ wqkv).reshape(B, S, 3, H, Dh), 2, 0, 3, 1, 4)
        q, k, v = qkv[0], qkv[1], qkv[2]
        scale = Dh ** -0.5
        pos = torch.arange(S)
        off = torch.arange(W) * dil
        idx = pos[:, None] - off[None, :]
        mask = idx >= 0
        idx_c = torch.where(mask, idx, 0).reshape(-1)
        kg = k[:, :, idx_c, :].reshape(B, H, S, W, Dh)
        vg = v[:, :, idx_c, :].reshape(B, H, S, W, Dh)
        logits = torch.einsum("bhsd,bhswd->bhsw", q, kg) * scale
        logits = torch.where(mask[None, None, :, :], logits, -1e9)
        attn = _softmax(logits, axis=-1)
        o = torch.einsum("bhsw,bhswd->bhsd", attn, vg)
        return (lambda x, *d: x.permute(*d))(o, 0, 2, 1, 3).reshape(B, S, D) @ wo

    def _B_ref(x, wqkv, w_phi, wo):
        B, S, D = x.shape
        H = 16; Dh = D // H
        qkv = (lambda x, *d: x.permute(*d))((x @ wqkv).reshape(B, S, 3, H, Dh), 2, 0, 3, 1, 4)
        q, k, v = qkv[0], qkv[1], qkv[2]
        pq = torch.einsum("bhsd,hdf->bhsf", q, w_phi)
        pk = torch.einsum("bhsd,hdf->bhsf", k, w_phi)
        phi_q = torch.concatenate([_softmax(pq, axis=-1), _softmax(-pq, axis=-1)], axis=-1)
        phi_k = torch.concatenate([_softmax(pk, axis=-1), _softmax(-pk, axis=-1)], axis=-1)
        kv = torch.einsum("bhsf,bhsd->bhfd", phi_k, v)
        z  = torch.einsum("bhsf->bhf", phi_k)
        num = torch.einsum("bhqf,bhfd->bhqd", phi_q, kv)
        den = torch.einsum("bhqf,bhf->bhq", phi_q, z)[..., None] + 1e-6
        o = num / den
        return (lambda x, *d: x.permute(*d))(o, 0, 2, 1, 3).reshape(B, S, D) @ wo

    x = A_x
    g = _sigmoid((x * gate_w).sum(-1, keepdims=True))
    a = _A_ref(x, A_wqkv, A_wo)
    b = _B_ref(x, B_wqkv, B_w_phi, B_wo)
    return g * a + (1 - g) * b
```

```cpp
#include <hip/hip_runtime.h>
#include <hip/hip_bf16.h>

typedef unsigned short u16;
static __device__ __forceinline__ float bf2f(u16 v) { return __uint_as_float(((unsigned)v) << 16); }
static __device__ __forceinline__ u16 f2bf(float f) { __hip_bfloat16 b = __float2bfloat16(f); return *reinterpret_cast<u16*>(&b); }
static __device__ __forceinline__ float rbf(float f) { return bf2f(f2bf(f)); }

constexpr int S = 2048, D = 448, H = 16, DH = 28, N3 = 1344;

__global__ void k_gemm(const u16* x, const u16* W, u16* C, int M, int N, int K) {
    int n = blockIdx.x * blockDim.x + threadIdx.x;
    int m = blockIdx.y;
    if (n >= N) return;
    float acc = 0.f;
    for (int k = 0; k < K; ++k) acc += bf2f(x[m * K + k]) * bf2f(W[k * N + n]);
    C[m * N + n] = f2bf(acc);
}

__global__ void k_attnA(const u16* qkv, u16* o) {
    int idx = blockIdx.x * blockDim.x + threadIdx.x;
    if (idx >= S * H) return;
    int s = idx / H, h = idx % H;
    const float scale = rbf(0.18898223650461361f);
    float lg[8];
    float mx = -1e30f;
    for (int w = 0; w < 8; ++w) {
        int p = s - 2 * w;
        float l;
        if (p >= 0) {
            float a = 0.f;
            for (int d = 0; d < DH; ++d) a += bf2f(qkv[s * N3 + h * DH + d]) * bf2f(qkv[p * N3 + D + h * DH + d]);
            l = rbf(rbf(a) * 0.18898223650461361f);
        } else l = -1e9f;
        lg[w] = l; mx = fmaxf(mx, l);
    }
    float sum = 0.f;
    for (int w = 0; w < 8; ++w) { lg[w] = __expf(lg[w] - mx); sum += lg[w]; }
    for (int w = 0; w < 8; ++w) lg[w] = rbf(lg[w] / sum);
    for (int d = 0; d < DH; ++d) {
        float a = 0.f;
        for (int w = 0; w < 8; ++w) { int p = s - 2 * w; if (p < 0) p = 0; a += lg[w] * bf2f(qkv[p * N3 + 2 * D + h * DH + d]); }
        o[s * D + h * DH + d] = f2bf(a);
    }
    (void)scale;
}

__global__ void k_phi(const u16* qkv, const u16* wphi, u16* phi) {
    int idx = blockIdx.x * blockDim.x + threadIdx.x;
    if (idx >= S * H * 2) return;
    int which = idx / (S * H); int r = idx % (S * H);
    int s = r / H, h = r % H;
    u16* outp = phi + (size_t)which * S * H * 56 + (size_t)(s * H + h) * 56;
    float mx = -1e30f, mn = 1e30f;
    for (int f = 0; f < DH; ++f) {
        float a = 0.f;
        for (int d = 0; d < DH; ++d) a += bf2f(qkv[s * N3 + which * D + h * DH + d]) * bf2f(wphi[(h * DH + d) * DH + f]);
        a = rbf(a);
        outp[f] = f2bf(a);
        mx = fmaxf(mx, a); mn = fminf(mn, a);
    }
    float s1 = 0.f, s2 = 0.f;
    for (int f = 0; f < DH; ++f) { float a = bf2f(outp[f]); s1 += __expf(a - mx); s2 += __expf(mn - a); }
    for (int f = 0; f < DH; ++f) { float a = bf2f(outp[f]); outp[28 + f] = f2bf(__expf(mn - a) / s2); outp[f] = f2bf(__expf(a - mx) / s1); }
}

__global__ void k_kv(const u16* qkv, const u16* phik, float* kv) {
    int idx = blockIdx.x * blockDim.x + threadIdx.x;
    if (idx >= H * 56 * 29) return;
    int d = idx % 29; int f = (idx / 29) % 56; int h = idx / (29 * 56);
    float a = 0.f;
    for (int s = 0; s < S; ++s) {
        float p = bf2f(phik[(size_t)(s * H + h) * 56 + f]);
        float v = d < 28 ? bf2f(qkv[s * N3 + 2 * D + h * DH + d]) : 1.f;
        a += p * v;
    }
    kv[idx] = rbf(a);
}

__global__ void k_oB(const u16* phiq, const float* kv, u16* o) {
    int idx = blockIdx.x * blockDim.x + threadIdx.x;
    if (idx >= S * H) return;
    int s = idx / H, h = idx % H;
    const u16* p = phiq + (size_t)(s * H + h) * 56;
    float den = 0.f;
    for (int f = 0; f < 56; ++f) den += bf2f(p[f]) * kv[(h * 56 + f) * 29 + 28];
    den = rbf(rbf(den) + 1e-6f);
    for (int d = 0; d < DH; ++d) {
        float a = 0.f;
        for (int f = 0; f < 56; ++f) a += bf2f(p[f]) * kv[(h * 56 + f) * 29 + d];
        o[s * D + h * DH + d] = f2bf(rbf(a) / den);
    }
}

__global__ void k_comb(const u16* x, const u16* gw, const u16* a, const u16* b, u16* out) {
    int s = blockIdx.x;
    __shared__ float red[64];
    float acc = 0.f;
    for (int k = threadIdx.x; k < D; k += 64) acc += rbf(bf2f(x[s * D + k]) * bf2f(gw[k]));
    red[threadIdx.x] = acc;
    __syncthreads();
    float t = 0.f;
    for (int i = 0; i < 64; ++i) t += red[i];
    t = rbf(t);
    float g = rbf(1.f / (1.f + __expf(-t)));
    float og = rbf(1.f - g);
    for (int k = threadIdx.x; k < D; k += 64) {
        float r = rbf(g * bf2f(a[s * D + k])) + rbf(og * bf2f(b[s * D + k]));
        out[s * D + k] = f2bf(r);
    }
}

extern "C" void kernel_launch(void* const* d_in, const int* in_sizes, int n_in,
                              void* d_out, int out_size, void* d_ws, size_t ws_size,
                              hipStream_t stream) {
    const u16* x = (const u16*)d_in[0];
    const u16* Aw = (const u16*)d_in[1];
    const u16* Awo = (const u16*)d_in[2];
    const u16* Bw = (const u16*)d_in[4];
    const u16* Bphi = (const u16*)d_in[5];
    const u16* Bwo = (const u16*)d_in[6];
    const u16* gw = (const u16*)d_in[7];
    u16* out = (u16*)d_out;
    char* ws = (char*)d_ws;
    u16* qkvA = (u16*)ws; ws += (size_t)S * N3 * 2;
    u16* qkvB = (u16*)ws; ws += (size_t)S * N3 * 2;
    u16* oA = (u16*)ws; ws += (size_t)S * D * 2;
    u16* oB = (u16*)ws; ws += (size_t)S * D * 2;
    u16* a = (u16*)ws; ws += (size_t)S * D * 2;
    u16* b = (u16*)ws; ws += (size_t)S * D * 2;
    u16* phi = (u16*)ws; ws += (size_t)2 * S * H * 56 * 2;
    float* kv = (float*)ws;
    k_gemm<<<dim3((N3 + 63) / 64, S), 64, 0, stream>>>(x, Aw, qkvA, S, N3, D);
    k_gemm<<<dim3((N3 + 63) / 64, S), 64, 0, stream>>>(x, Bw, qkvB, S, N3, D);
    k_attnA<<<(S * H + 63) / 64, 64, 0, stream>>>(qkvA, oA);
    k_phi<<<(S * H * 2 + 63) / 64, 64, 0, stream>>>(qkvB, Bphi, phi);
    k_kv<<<(H * 56 * 29 + 63) / 64, 64, 0, stream>>>(qkvB, phi + (size_t)S * H * 56, kv);
    k_oB<<<(S * H + 63) / 64, 64, 0, stream>>>(phi, kv, oB);
    k_gemm<<<dim3((D + 63) / 64, S), 64, 0, stream>>>(oA, Awo, a, S, D, D);
    k_gemm<<<dim3((D + 63) / 64, S), 64, 0, stream>>>(oB, Bwo, b, S, D, D);
    k_comb<<<S, 64, 0, stream>>>(x, gw, a, b, out);
}
```

```cpp
#include <hip/hip_runtime.h>
#include <stdint.h>

typedef unsigned u32;
typedef unsigned long long u64;
typedef u32 u32x4 __attribute__((ext_vector_type(4)));
typedef u32 u32x2 __attribute__((ext_vector_type(2)));
typedef float f32x2 __attribute__((ext_vector_type(2)));
typedef float f32x4 __attribute__((ext_vector_type(4)));
typedef float f32x16 __attribute__((ext_vector_type(16)));
typedef __bf16 bf16x8 __attribute__((ext_vector_type(8)));
typedef __bf16 bf16x2 __attribute__((ext_vector_type(2)));
typedef short s16x4 __attribute__((ext_vector_type(4)));
typedef __amdgpu_buffer_rsrc_t rsrc_t;
#define LDSP(T) __attribute__((address_space(3))) T*
#define DI __device__ __forceinline__

constexpr u32 WS_E0 = 0;
constexpr u32 WS_FL = 256;
constexpr u32 WS_KF = 4096;
constexpr u32 WS_G = 8192;
constexpr u32 WS_KVR = 16384;
constexpr u32 WS_KX = 256 * 1024;
constexpr u32 WS_P = 1024 * 1024;
constexpr u32 WS_OA = 3 * 1024 * 1024;
constexpr u32 WS_OB = 5 * 1024 * 1024;
constexpr u32 WS_BYTES = 7 * 1024 * 1024;
constexpr u32 L_WQ = 104 * 1024, L_WOA = 48 * 1024, L_WOB = 104 * 1024;
constexpr u32 L_PHI = 0, L_VP = 32 * 1024, L_WPHI = 128 * 1024, L_JUNK = 144 * 1024;
constexpr u32 L_AX = 0, L_KVL = 0, L_XO = 8 * 1024;
constexpr u32 OOB = 0x80000000u;
constexpr int SPIN = 1 << 15;
constexpr int SC1 = 16;
constexpr float LOG2E = 1.4426950408889634f;

DI void dma(u32 m0v, u32 voff, rsrc_t rs, u32 soff) {
    asm volatile("s_mov_b32 m0, %0\n\ts_nop 0\n\tbuffer_load_dwordx4 %1, %2, %3 offen lds" ::"s"(m0v), "v"(voff), "s"(rs), "s"(soff) : "memory");
}
#define WAITBAR(N) asm volatile("s_waitcnt vmcnt(" #N ") lgkmcnt(0)\n\ts_barrier" ::: "memory")
#define LBAR() asm volatile("s_waitcnt lgkmcnt(0)\n\ts_barrier" ::: "memory")
#define WAITVM(N) asm volatile("s_waitcnt vmcnt(" #N ")" ::: "memory")
#define SCHED() __builtin_amdgcn_sched_barrier(0)
DI u32x4 r128(u32 a) { return *(LDSP(const u32x4))(size_t)a; }
DI void w128(u32 a, u32x4 v) { *(LDSP(u32x4))(size_t)a = v; }
DI void w64(u32 a, u32x2 v) { *(LDSP(u32x2))(size_t)a = v; }
DI u32x2 rtr(u32 a) { return __builtin_bit_cast(u32x2, __builtin_amdgcn_ds_read_tr16_b64_v4i16((LDSP(s16x4))(size_t)a)); }
DI u32x4 rtr2(u32 a, u32 b) { u32x2 x = rtr(a), y = rtr(b); return u32x4{x.x, x.y, y.x, y.y}; }
DI f32x16 mm32(u32x4 a, u32x4 b, f32x16 c) { return __builtin_amdgcn_mfma_f32_32x32x16_bf16(__builtin_bit_cast(bf16x8, a), __builtin_bit_cast(bf16x8, b), c, 0, 0, 0); }
DI f32x4 mm16(u32x4 a, u32x4 b, f32x4 c) { return __builtin_amdgcn_mfma_f32_16x16x32_bf16(__builtin_bit_cast(bf16x8, a), __builtin_bit_cast(bf16x8, b), c, 0, 0, 0); }
DI u32 pk(float lo, float hi) { f32x2 v = {lo, hi}; return __builtin_bit_cast(u32, __builtin_convertvector(v, bf16x2)); }
DI u32x4 pk8(const f32x16& a, int s) { return u32x4{pk(a[8 * s], a[8 * s + 1]), pk(a[8 * s + 2], a[8 * s + 3]), pk(a[8 * s + 4], a[8 * s + 5]), pk(a[8 * s + 6], a[8 * s + 7])}; }
DI float f_(u32 x) { return __builtin_bit_cast(float, x); }
DI u32 u_(float x) { return __builtin_bit_cast(u32, x); }
DI float xmax32(float x) { auto r = __builtin_amdgcn_permlane32_swap(u_(x), u_(x), false, false); return fmaxf(f_(r[0]), f_(r[1])); }
DI float xmin32(float x) { auto r = __builtin_amdgcn_permlane32_swap(u_(x), u_(x), false, false); return fminf(f_(r[0]), f_(r[1])); }
DI float xadd32(float x) { auto r = __builtin_amdgcn_permlane32_swap(u_(x), u_(x), false, false); return f_(r[0]) + f_(r[1]); }
DI u32x4 gld(rsrc_t rs, u32 off) { return __builtin_amdgcn_raw_buffer_load_b128(rs, off, 0, SC1); }
DI void gst(rsrc_t rs, u32 off, u32x4 v) { __builtin_amdgcn_raw_buffer_store_b128(v, rs, off, 0, SC1); }
DI void gst64(rsrc_t rs, u32 off, u32x2 v) { __builtin_amdgcn_raw_buffer_store_b64(v, rs, off, 0, SC1); }
DI u32 opq(u32 x) { asm("" : "+v"(x)); return x; }
DI float sel(u32 m, float a, float b) { return f_((u_(a) & m) | (u_(b) & ~m)); }
DI void nap() { asm volatile("s_sleep 1" ::: "memory"); }
DI bool none(bool bad) { return __builtin_amdgcn_ballot_w64(bad) == 0; }
DI rsrc_t mk(const void* p, u32 bytes) { return __builtin_amdgcn_make_buffer_rsrc((void*)p, 0, bytes, 0x00020000); }
DI void wait_flags(rsrc_t rs, u32 off, u32x2 tag) {
#pragma nounroll
    for (int it = 0; it < SPIN; ++it) {
        u32x2 v = __builtin_amdgcn_raw_buffer_load_b64(rs, off, 0, SC1);
        if (none(off != OOB && (v.x != tag.x || v.y != tag.y))) break;
        nap();
    }
    asm volatile("" ::: "memory");
}

DI void phi(const f32x16& p, float keep, u32x4 (&o)[4]) {
    float mx = fmaxf(p[0], p[1]), mn = fminf(p[0], p[1]);
#pragma unroll
    for (int i = 2; i < 16; i += 2) { mx = fmaxf(fmaxf(mx, p[i]), p[i + 1]); mn = fminf(fminf(mn, p[i]), p[i + 1]); }
    mx = xmax32(mx) * -LOG2E; mn = xmin32(mn) * LOG2E;
    f32x16 a, b;
#pragma unroll
    for (int i = 0; i < 16; ++i) { a[i] = __builtin_amdgcn_exp2f(fmaf(p[i], LOG2E, mx)); b[i] = __builtin_amdgcn_exp2f(fmaf(p[i], -LOG2E, mn)); }
    float sa = ((a[12] + a[13]) + (a[14] + a[15])) * keep, sb = ((b[12] + b[13]) + (b[14] + b[15])) * keep;
#pragma unroll
    for (int i = 0; i < 12; i += 4) { sa += (a[i] + a[i + 1]) + (a[i + 2] + a[i + 3]); sb += (b[i] + b[i + 1]) + (b[i + 2] + b[i + 3]); }
    sa = __builtin_amdgcn_rcpf(xadd32(sa)); sb = __builtin_amdgcn_rcpf(xadd32(sb));
    a *= sa; b *= sb;
    o[0] = pk8(a, 0); o[1] = pk8(a, 1); o[2] = pk8(b, 0); o[3] = pk8(b, 1);
}

#define STAMP(i)

__global__ __launch_bounds__(512, 2) void fused(const void* __restrict__ gx, const void* __restrict__ gwa, const void* __restrict__ gwoa,
                                                const void* __restrict__ gwb, const void* __restrict__ gwphi, const void* __restrict__ gwob,
                                                const void* __restrict__ ggate, void* gout, void* gws, const u64* __restrict__ gepoch) {
    __shared__ __attribute__((aligned(1024))) char smem[163840];
    STAMP(0);
    const u32 tid = threadIdx.x, l = tid & 63;
    const u32 w = __builtin_amdgcn_readfirstlane(tid >> 6);
    const u32 lb = (u32)(size_t)(LDSP(char))smem;
    {
        u64 pc; u32 len;
        asm volatile("s_getpc_b64 %0\n.Lfm_begin:\n\ts_mov_b32 %1, .Lfm_end-.Lfm_begin\n\ts_max_i32 %1, %1, 0\n\ts_nop 4" : "=s"(pc), "=s"(len)::"scc");
        dma(lb + L_JUNK + w * 1024, tid * 128, mk((const void*)pc, len), 0);
    }
    const u32 b = blockIdx.x, xcd = b & 7, bi = b >> 3;
    const bool isA = bi >= 16;
    const u32 t = (xcd >> 1) * 4 + ((bi & 15) >> 2), j = (xcd & 1) * 4 + (bi & 3);
    const u32 rg = w & 3, hh = w >> 2, head = 2 * j + hh;
    const u32 tok = l & 31, h = l >> 5, p = l & 15, g = l >> 4;
    const rsrc_t rx = mk(gx, 2048 * 896), rw = mk(isA ? gwa : gwb, 448 * 2688), rs = mk(gws, WS_BYTES);

    const u32 vx0 = (t * 128 + w * 16 + (l >> 3)) * 896 + (((l & 7) ^ g) << 4);
    const u32 vx1 = (vx0 + 8 * 896) ^ 64;
    const u32 wsrc = (l & 7) ^ ((g & 1) << 2);
    const u32 vw = wsrc == 7 ? OOB : (w * 8 + (l >> 3)) * 2688 + j * 112 + wsrc * 16;
    u32 gatev;
    asm volatile("buffer_load_ushort %0, %1, %2, 0 offen" : "=v"(gatev) : "v"(tid * 2), "s"(mk(ggate, 896)) : "memory");
    if (!isA) {
        const rsrc_t rp = mk(gwphi, 16 * 1568);
        dma(lb + L_WPHI + w * 2048, l * 16, rp, head * 1568);
        dma(lb + L_WPHI + w * 2048 + 1024, l < 34 ? l * 16 + 1024 : OOB, rp, head * 1568);
    }
    auto chunk = [&](u32 c) {
        const u32 sl = lb + (c & 3) * 32768;
        dma(sl + w * 2048, vx0, rx, c * 128);
        dma(sl + w * 2048 + 1024, vx1, rx, c * 128);
        dma(sl + 16384 + w * 1024, vw, rw, c * (64 * 2688) + 896);
        dma(sl + 24576 + w * 1024, vw, rw, c * (64 * 2688) + 1792);
    };
    chunk(0); chunk(1); chunk(2); chunk(3);

    const u64 e0 = *gepoch;
    const u64 tg = e0 + 0x9E3779B97F4A7C15ull;
    const u32x2 tag = {(u32)tg, (u32)(tg >> 32)};

    const u32 xa = lb + (rg * 32 + tok) * 128 + ((h ^ ((tok >> 1) & 7)) << 4);
    const u32 wa = lb + (8 * h + (p >> 2)) * 128 + ((56 * hh + 32 * (g & 1) + 8 * (p & 3)) ^ (((p >> 3) & 1) << 6));

    const u32 xah = opq(xa + 65536), wah = opq(wa + 65536);
    u32x4 xf[28], wphi[2];
    f32x16 ak = {}, av = {};
    u32x4 wk[4], wv[4];
    auto rd = [&](int c, int s) {
        const u32 sl = (c & 1) * 32768, xb = (c & 2) ? xah : xa, wb = (c & 2) ? wah : wa;
        xf[4 * c + s] = r128((xb ^ (32 * s)) + sl);
        wk[s] = rtr2(wb + sl + 16384 + s * 2048, wb + sl + 16384 + s * 2048 + 512);
        wv[s] = rtr2(wb + sl + 24576 + s * 2048, wb + sl + 24576 + s * 2048 + 512);
    };
    auto mm = [&](int c, int s) { ak = mm32(wk[s], xf[4 * c + s], ak); av = mm32(xf[4 * c + s], wv[s], av); };
#pragma unroll
    for (int c = 0; c < 7; ++c) {
        if (c == 0) WAITBAR(12); else if (c < 5) WAITBAR(8); else if (c == 5) WAITBAR(11); else WAITBAR(7);
        if (c == 0) STAMP(1);
        rd(c, 0); rd(c, 1);
        if (c == 0 && !isA) {
            const u32 fq = 4 * (g & 1) + (p & 3);
            const u32 pa = lb + L_WPHI + w * 2048 + (4 * h + (p >> 2)) * 56 + (fq == 7 ? 6 : fq) * 8;
            wphi[0] = rtr2(pa, pa + 8 * 56); wphi[1] = rtr2(pa + 16 * 56, pa + 24 * 56);
        }
        SCHED();
        if (c >= 1 && c <= 3) chunk(c + 3);
        if (c == 4) {
#pragma unroll
            for (u32 q = 0; q < 7; ++q) dma(lb + L_WQ + q * 8192 + w * 1024, vw, rw, q * (64 * 2688));
        }
        if (c > 0) { mm(c - 1, 2); mm(c - 1, 3); }
        SCHED();
        rd(c, 2); rd(c, 3);
        SCHED();
        mm(c, 0); mm(c, 1);
        SCHED();
    }
    mm(6, 2); mm(6, 3);
    asm volatile("" : "+v"(gatev));
    STAMP(2);

    const u32 i2 = bi;
    const bool p2 = i2 < 28;
    const u32 u2 = xcd * 4 + i2 / 7, cb = i2 % 7;
    const u32 vwo = (w * 8 + (l >> 3)) * 896 + cb * 128 + (((l & 7) ^ (g << 1)) << 4);
    auto wo_dma = [&](const void* gp, u32 dst) {
        if (p2) {
            const rsrc_t ro = mk(gp, 448 * 896);
#pragma unroll
            for (u32 q = 0; q < 7; ++q) dma(lb + dst + q * 8192 + w * 1024, vwo, ro, q * (64 * 896));
        }
    };
    auto stage2 = [&]() {
        f32x16 aq = {};
        const u32 wq = opq(wa + L_WQ);
#pragma unroll
        for (int ks = 0; ks < 28; ++ks) aq = mm32(rtr2(wq + ks * 2048, wq + ks * 2048 + 512), xf[ks], aq);
        return aq;
    };
    const u32 ooff = ((t * 8 + rg * 2 + (tok >> 4)) * 16 + head) * 1024 + h * 256 + p * 16;
    auto publish_o = [&](f32x16 o, float r, u32 base) {
        o *= r;
        u32x4 o0 = pk8(o, 0), o1 = pk8(o, 1);
        if (h) { o1.z = 0; o1.w = 0; }
        gst(rs, base + ooff, o0); gst(rs, base + ooff + 512, o1);
    };
    const u32x4 kf0 = pk8(ak, 0), kf1 = pk8(ak, 1);
    u32x4 vf0 = pk8(av, 0), vf1 = pk8(av, 1);

    if (!isA) {
        const float keep = h ? 0.f : 1.f;
        {
            f32x16 pp = mm32(wphi[0], kf0, f32x16{});
            pp = mm32(wphi[1], kf1, pp);
            if (tok == 28) { vf0 = u32x4{0x3F803F80u, 0x3F803F80u, 0x3F803F80u, 0x3F803F80u}; vf1 = vf0; }
            w128(lb + L_VP + w * 2048 + l * 16, vf0); w128(lb + L_VP + w * 2048 + 1024 + l * 16, vf1);
            u32x4 ph[4];
            phi(pp, keep, ph);
            const u32 F = (((tok >> 1) & 3) << 2) | (((tok >> 3) & 1) << 1) | (tok & 1);
            const u32 pw = lb + L_PHI + w * 4096 + tok * 128 + ((h ^ F) << 3);
#pragma unroll
            for (int s = 0; s < 4; ++s) {
                const u32 cu = 8 * (s >> 1) + 2 * (s & 1);
                w64(pw ^ (cu << 3), u32x2{ph[s].x, ph[s].y});
                w64(pw ^ ((cu + 4) << 3), u32x2{ph[s].z, ph[s].w});
            }
        }
        WAITBAR(0);
        STAMP(3);
        {
            const u32 sign = rg >> 1, dt = rg & 1;
            const u32 F = ((2 * (g & 1) + (p >> 3)) << 2) | ((p >> 2) & 1);
            const u32 pr = lb + L_PHI + hh * 16384 + (16 * (g >> 1) + 4 * (g & 1) + (p >> 2)) * 128 + (((8 * sign + (p & 3)) ^ F) << 3);
            const u32 vr = lb + L_VP + hh * 8192 + g * 512 + (16 * dt + p) * 16;
            f32x4 c0 = {}, c1 = {};
#pragma unroll
            for (int r = 0; r < 4; ++r) {
                const u32x4 vv = r128(vr + r * 2048);
                c0 = mm16(rtr2(pr + r * 4096, (pr ^ 16) + r * 4096 + 1024), vv, c0);
                c1 = mm16(rtr2((pr ^ 32) + r * 4096, (pr ^ 48) + r * 4096 + 1024), vv, c1);
            }
            const u32 t0 = tag.x & 0xFFF, t1 = (tag.x >> 12) & 0xFFF, t2 = tag.y & 0xFFF, t3 = (tag.y >> 12) & 0xFFF;
            const u32 po = WS_P + (j * 8 + w) * 32768 + t * 1024 + l * 16;
#define TG(x, tt) (((u_(x) + 0x800u) & 0xFFFFF000u) | (tt))
            gst(rs, po, u32x4{TG(c0[0], t0), TG(c0[1], t1), TG(c0[2], t2), TG(c0[3], t3)});
            gst(rs, po + 16384, u32x4{TG(c1[0], t0), TG(c1[1], t1), TG(c1[2], t2), TG(c1[3], t3)});
        }
        STAMP(4);
        const u32x4 ones = {~0u, ~0u, ~0u, ~0u};
        gst(rs, WS_OB + ooff, ones); gst(rs, WS_OB + ooff + 512, ones);
        wo_dma(gwoa, L_WOA);
        u32x4 pq[4];
        {
            const f32x16 aq = stage2();
            f32x16 pp = mm32(wphi[0], pk8(aq, 0), f32x16{});
            pp = mm32(wphi[1], pk8(aq, 1), pp);
            phi(pp, keep, pq);
        }
        WAITBAR(0);
        STAMP(5);
        gst64(rs, tid == 0 ? WS_FL + (t * 16 + 8 + j) * 8 : OOB, tag);
        {
            const u32 un = t >> 1, cg = (t & 1) * 8 + w;
            const u32 po = WS_P + (j * 8 + un) * 32768 + (l >> 2) * 1024 + (4 * cg + (l & 3)) * 16;
            u32x4 a, bq;
        #pragma nounroll
    for (int it = 0; it < SPIN; ++it) {
                a = gld(rs, po); bq = gld(rs, po + 16384);
                const u32 bad = ((a.x ^ tag.x) | (bq.x ^ tag.x)) | ((a.y ^ (tag.x >> 12)) | (bq.y ^ (tag.x >> 12))) |
                                ((a.z ^ tag.y) | (bq.z ^ tag.y)) | ((a.w ^ (tag.y >> 12)) | (bq.w ^ (tag.y >> 12)));
                if (none((bad & 0xFFF) != 0)) break;
                nap();
            }
            STAMP(6);
            const u32 gq = cg >> 2;
            const u32 mb = gq == 3 ? 0u : 0xFFFFF000u;
            float uu[4];
#pragma unroll
            for (int i = 0; i < 4; ++i) {
                auto r = __builtin_amdgcn_permlane32_swap(a[i] & 0xFFFFF000u, bq[i] & mb, false, false);
                uu[i] = f_(r[0]) + f_(r[1]);
            }
            float ww[2];
#pragma unroll
            for (int i = 0; i < 2; ++i) {
                auto r = __builtin_amdgcn_permlane16_swap(u_(uu[i]), u_(uu[i + 2]), false, false);
                float y = f_(r[0]) + f_(r[1]);
                y += f_(__builtin_amdgcn_update_dpp(0, u_(y), 0x128, 0xf, 0xf, false));
                y += f_(__builtin_amdgcn_update_dpp(0, u_(y), 0x124, 0xf, 0xf, false));
                ww[i] = y;
            }
            const u32 pp = pk(ww[0], ww[1]), t16 = tag.x & 0xFFFF;
            const u32 s = ((un >> 1) & 1) * 2 + (gq >> 1);
            const u32 l32 = 16 * (un & 1) + ((4 * cg + (l & 3)) & 15) + 32 * (gq & 1);
            const u32 ko = WS_KVR + (((2 * j + (un >> 2)) * 4 + s) * 2 + (g >> 1)) * 1024 + l32 * 16 + (g & 1) * 8;
            gst64(rs, (l & 12) == 0 ? ko : OOB, u32x2{(pp << 16) | t16, (pp & 0xFFFF0000u) | t16});
        }
        wo_dma(gwob, L_WOB);
        {
            const u32 ko = WS_KVR + (head * 4 + rg) * 2048 + l * 16;
            u32x4 a, bq;
        #pragma nounroll
    for (int it = 0; it < SPIN; ++it) {
                a = gld(rs, ko); bq = gld(rs, ko + 1024);
                const u32 bad = ((a.x ^ tag.x) | (bq.x ^ tag.x)) | ((a.y ^ tag.x) | (bq.y ^ tag.x)) | ((a.z ^ tag.x) | (bq.z ^ tag.x)) | ((a.w ^ tag.x) | (bq.w ^ tag.x));
                if (none((bad & 0xFFFF) != 0)) break;
                nap();
            }
            STAMP(7);
#define HI2(x, y) __builtin_amdgcn_perm(y, x, 0x07060302u)
            w128(lb + L_KVL + w * 1024 + l * 16, u32x4{HI2(a.x, a.y), HI2(a.z, a.w), HI2(bq.x, bq.y), HI2(bq.z, bq.w)});
            LBAR();
            f32x16 o = {};
#pragma unroll
            for (int s = 0; s < 4; ++s) o = mm32(r128(lb + L_KVL + (hh * 4 + s) * 1024 + l * 16), pq[s], o);
            auto r = __builtin_amdgcn_permlane32_swap(u_(o[12]), u_(o[12]), false, false);
            publish_o(o, __builtin_amdgcn_rcpf(f_(r[1]) + 1e-6f), WS_OB);
        }
        STAMP(8);
    } else {
        const u32 kxo = WS_KX + (t * 16 + head) * 3072 + l * 16;
        if (rg < 3) {
            const u32 ax = lb + L_AX + w * 3072 + l * 16;
            w128(ax, kf0); w128(ax + 1024, kf1); w128(ax + 2048, vf1);
        } else {
            gst(rs, kxo, kf0); gst(rs, kxo + 1024, kf1); gst(rs, kxo + 2048, vf1);
            WAITVM(0);
            gst64(rs, l == 0 ? WS_KF + (t * 16 + head) * 8 : OOB, tag);
        }
        WAITBAR(0);
        STAMP(3);
        if (j == 0) {
            if (tid < 448) w128(lb + L_WQ + tid * 128 + ((7 ^ ((tid & 2) << 1)) << 4), u32x4{gatev, 0, 0, 0});
            LBAR();
        }
        wo_dma(gwoa, L_WOA);
        const f32x16 aq = stage2();
        STAMP(4);
        if (j == 0 && hh == 1) {
            const float gg = __builtin_amdgcn_rcpf(1.f + __builtin_amdgcn_exp2f(aq[12] * -LOG2E));
            __builtin_amdgcn_raw_buffer_store_b32(u_(gg), rs, h ? WS_G + (t * 128 + rg * 32 + tok) * 4 : OOB, 0, SC1);
        }
        u32x4 qf0 = pk8(aq, 0), qf1 = pk8(aq, 1);
        if (h) { qf1.z = 0; qf1.w = 0; }
        u32x4 kp0, kp1, vp;
        bool hasprev = true;
        if (rg > 0) {
            const u32 ax = lb + L_AX + (w - 1) * 3072 + l * 16;
            kp0 = r128(ax); kp1 = r128(ax + 1024); vp = r128(ax + 2048);
        } else if (t > 0) {
            wait_flags(rs, l == 0 ? WS_KF + ((t - 1) * 16 + head) * 8 : OOB, tag);
            kp0 = gld(rs, kxo - 16 * 3072); kp1 = gld(rs, kxo - 16 * 3072 + 1024); vp = gld(rs, kxo - 16 * 3072 + 2048);
        } else {
            kp0 = u32x4{}; kp1 = u32x4{}; vp = u32x4{}; hasprev = false;
        }
        STAMP(5);
        f32x16 so = mm32(kf0, qf0, f32x16{}); so = mm32(kf1, qf1, so);
        f32x16 sp = mm32(kp0, qf0, f32x16{}); sp = mm32(kp1, qf1, sp);
        const u32 om = opq(0u - (tok & 1));
        const u32 base = (tok & ~1u) - 4 * h, basep = hasprev ? base + 32 : 1000u;
        float sv[12];
        const float NEG = -1e30f;
#pragma unroll
        for (int i = 0; i < 8; ++i) { const float v = sel(om, so[2 * i + 1], so[2 * i]); sv[i] = (base - (8 * (i >> 1) + 2 * (i & 1))) <= 14u ? v : NEG; }
#pragma unroll
        for (int i = 4; i < 8; ++i) { const float v = sel(om, sp[2 * i + 1], sp[2 * i]); sv[4 + i] = (basep - (8 * (i >> 1) + 2 * (i & 1))) <= 14u ? v : NEG; }
        float mx = fmaxf(sv[0], sv[1]);
#pragma unroll
        for (int i = 2; i < 12; i += 2) mx = fmaxf(fmaxf(mx, sv[i]), sv[i + 1]);
        const float sc = 0.18898223650461362f * LOG2E;
        mx = xmax32(mx) * -sc;
        float sum = 0.f;
        u32 pd[12];
        const u32 pm = om ^ 0x0000FFFFu;
#pragma unroll
        for (int i = 0; i < 12; ++i) { const float e = __builtin_amdgcn_exp2f(fmaf(sv[i], sc, mx)); sum += e; pd[i] = pk(e, e) & pm; }
        f32x16 o = mm32(vf0, u32x4{pd[0], pd[1], pd[2], pd[3]}, f32x16{});
        o = mm32(vf1, u32x4{pd[4], pd[5], pd[6], pd[7]}, o);
        o = mm32(vp, u32x4{pd[8], pd[9], pd[10], pd[11]}, o);
        publish_o(o, __builtin_amdgcn_rcpf(xadd32(sum)), WS_OA);
        STAMP(6);
        WAITBAR(0);
        gst64(rs, tid == 0 ? WS_FL + (t * 16 + j) * 8 : OOB, tag);
        wo_dma(gwob, L_WOB);
        STAMP(8);
    }

    if (p2) {
        const u32 tt = w & 3, kh = w >> 2;
        WAITBAR(0);
        wait_flags(rs, l < 16 ? WS_FL + ((u2 >> 1) * 16 + l) * 8 : OOB, tag);
        STAMP(9);
        const u32 oo = ((u2 * 4 + tt) * 16 + kh * 8) * 1024 + l * 16;
        u32x4 of[8];
#pragma unroll
        for (int e = 0; e < 8; ++e) of[e] = gld(rs, WS_OA + oo + e * 1024);
        const float gg = f_(__builtin_amdgcn_raw_buffer_load_b32(rs, WS_G + (u2 * 64 + tt * 16 + p) * 4, 0, SC1));
        const u32 f0 = 2 * (g & 1) + (p >> 3);
        const u32 wb0 = lb + (224 * kh + 16 * (g >> 1) + 4 * (g & 1) + (p >> 2)) * 128 + ((((p & 3) >> 1) ^ (2 * f0)) << 4) + (p & 1) * 8;
        const u32 wb1 = wb0 + (g == 3 ? 0 : 1024);
        u32 wr0[4], wr1[4];
        auto wbase = [&](u32 img) {
#pragma unroll
            for (int x = 0; x < 4; ++x) { wr0[x] = opq((wb0 + img) ^ (x << 5)); wr1[x] = opq((wb1 + img) ^ (x << 5)); }
        };
        auto wfrag = [&](int e, int nt) {
            const int x = nt ^ (2 * (e & 1));
            return rtr2(wr0[x] + e * 3584, wr1[x] + e * 3584);
        };
        wbase(L_WOA);
        f32x4 ca[4] = {}, cbb[4] = {};
#pragma unroll
        for (int e = 0; e < 8; ++e)
#pragma unroll
            for (int nt = 0; nt < 4; ++nt) ca[nt] = mm16(wfrag(e, nt), of[e], ca[nt]);
        STAMP(10);
        u32x4 wf[8][4];
        wbase(L_WOB);
#pragma unroll
        for (int e = 0; e < 8; ++e)
#pragma unroll
            for (int nt = 0; nt < 4; ++nt) wf[e][nt] = wfrag(e, nt);
    #pragma nounroll
    for (int it = 0; it < SPIN; ++it) {
            u32 m = 0;
#pragma unroll
            for (int e = 0; e < 8; ++e) of[e] = gld(rs, WS_OB + oo + e * 1024);
#pragma unroll
            for (int e = 0; e < 8; ++e) m = max(max(m, max(of[e].x, of[e].y)), max(of[e].z, of[e].w));
            if (none(m == ~0u)) break;
            nap();
        }
        STAMP(11);
#pragma unroll
        for (int e = 0; e < 8; ++e)
#pragma unroll
            for (int nt = 0; nt < 4; ++nt) cbb[nt] = mm16(wf[e][nt], of[e], cbb[nt]);
        const u32 xo = lb + L_XO + kh * 16384 + (tt * 16 + p) * 256 + ((g ^ (p & 7)) << 4);
#pragma unroll
        for (int nt = 0; nt < 4; ++nt) {
            const f32x4 r = ca[nt] * gg + cbb[nt] * (1.f - gg);
            w128(xo ^ (nt << 6), __builtin_bit_cast(u32x4, r));
        }
        LBAR();
        const u32 tk = tid >> 3, c8 = tid & 7;
        const u32 xr = lb + L_XO + tk * 256 + (((2 * c8) ^ (tk & 7)) << 4);
        const f32x4 r0 = __builtin_bit_cast(f32x4, r128(xr)) + __builtin_bit_cast(f32x4, r128(xr + 16384));
        const f32x4 r1 = __builtin_bit_cast(f32x4, r128(xr ^ 16)) + __builtin_bit_cast(f32x4, r128((xr ^ 16) + 16384));
        __builtin_amdgcn_raw_buffer_store_b128(u32x4{pk(r0[0], r0[1]), pk(r0[2], r0[3]), pk(r1[0], r1[1]), pk(r1[2], r1[3])},
                                               mk(gout, 2048 * 896), (u2 * 64 + tk) * 896 + cb * 128 + c8 * 16, 0, 0);
        STAMP(12);
    } else if (b == 255 && w < 4) {
        wait_flags(rs, WS_FL + tid * 8, tag);
        asm volatile("s_barrier" ::: "memory");
        gst64(rs, tid == 0 ? WS_E0 : OOB, tag);
    } else if (b == 255) {
        asm volatile("s_barrier" ::: "memory");
    }
    asm volatile(".Lfm_end:" ::: "memory");
}

extern "C" void kernel_launch(void* const* d_in, const int* in_sizes, int n_in, void* d_out, int out_size, void* d_ws, size_t ws_size, hipStream_t stream) {
    (void)in_sizes; (void)n_in; (void)out_size; (void)ws_size;
    hipLaunchKernelGGL(fused, dim3(256), dim3(512), 0, stream, d_in[0], d_in[1], d_in[2], d_in[4], d_in[5], d_in[6], d_in[7], d_out, d_ws, (const u64*)d_ws);
}
```

```cpp
#define ESEL 99
#define EONLYB 0
#define EK 0
#include <hip/hip_runtime.h>
#include <stdint.h>

typedef unsigned u32;
typedef unsigned long long u64;
typedef u32 u32x4 __attribute__((ext_vector_type(4)));
typedef u32 u32x2 __attribute__((ext_vector_type(2)));
typedef float f32x2 __attribute__((ext_vector_type(2)));
typedef float f32x4 __attribute__((ext_vector_type(4)));
typedef float f32x16 __attribute__((ext_vector_type(16)));
typedef __bf16 bf16x8 __attribute__((ext_vector_type(8)));
typedef __bf16 bf16x2 __attribute__((ext_vector_type(2)));
typedef short s16x4 __attribute__((ext_vector_type(4)));
typedef __amdgpu_buffer_rsrc_t rsrc_t;
#define LDSP(T) __attribute__((address_space(3))) T*
#define DI __device__ __forceinline__

constexpr u32 WS_E0 = 0;
constexpr u32 WS_FL = 256;
constexpr u32 WS_KF = 4096;
constexpr u32 WS_G = 8192;
constexpr u32 WS_KVR = 16384;
constexpr u32 WS_KX = 256 * 1024;
constexpr u32 WS_P = 1024 * 1024;
constexpr u32 WS_OA = 3 * 1024 * 1024;
constexpr u32 WS_OB = 5 * 1024 * 1024;
constexpr u32 WS_BYTES = 7 * 1024 * 1024;
constexpr u32 L_WQ = 104 * 1024, L_WOA = 48 * 1024, L_WOB = 104 * 1024;
constexpr u32 L_PHI = 0, L_VP = 32 * 1024, L_WPHI = 128 * 1024, L_JUNK = 144 * 1024;
constexpr u32 L_AX = 0, L_KVL = 0, L_XO = 8 * 1024;
constexpr u32 OOB = 0x80000000u;
constexpr int SPIN = 1 << 15;
constexpr int SC1 = 16;
#define ADELAY 3500
#define ADC 0
#define PRO 2
#define WARM 1
template <int N> struct ic { static constexpr int value = N; };
constexpr float LOG2E = 1.4426950408889634f;

DI void dma(u32 m0v, u32 voff, rsrc_t rs, u32 soff) {
    asm volatile("s_mov_b32 m0, %0\n\ts_nop 0\n\tbuffer_load_dwordx4 %1, %2, %3 offen lds" ::"s"(m0v), "v"(voff), "s"(rs), "s"(soff) : "memory");
}
DI void dma4(u32 m0v, u32 voff, rsrc_t rs) {
    asm volatile("s_mov_b32 m0, %0\n\ts_nop 0\n\tbuffer_load_dword %1, %2, 0 offen lds" ::"s"(m0v), "v"(voff), "s"(rs) : "memory");
}
template <int N> DI void waitbar() { asm volatile("s_waitcnt vmcnt(%0) lgkmcnt(0)\n\ts_barrier" ::"i"(N) : "memory"); }
#define WAITBAR(N) asm volatile("s_waitcnt vmcnt(" #N ") lgkmcnt(0)\n\ts_barrier" ::: "memory")
#define LBAR() asm volatile("s_waitcnt lgkmcnt(0)\n\ts_barrier" ::: "memory")
#define WAITVM(N) asm volatile("s_waitcnt vmcnt(" #N ")" ::: "memory")
#define SCHED() __builtin_amdgcn_sched_barrier(0)
DI u32x4 r128(u32 a) { return *(LDSP(const u32x4))(size_t)a; }
DI void w128(u32 a, u32x4 v) { *(LDSP(u32x4))(size_t)a = v; }
DI void w64(u32 a, u32x2 v) { *(LDSP(u32x2))(size_t)a = v; }
DI u32x2 rtr(u32 a) { return __builtin_bit_cast(u32x2, __builtin_amdgcn_ds_read_tr16_b64_v4i16((LDSP(s16x4))(size_t)a)); }
DI u32x4 rtr2(u32 a, u32 b) { u32x2 x = rtr(a), y = rtr(b); return u32x4{x.x, x.y, y.x, y.y}; }
DI f32x16 mm32(u32x4 a, u32x4 b, f32x16 c) { return __builtin_amdgcn_mfma_f32_32x32x16_bf16(__builtin_bit_cast(bf16x8, a), __builtin_bit_cast(bf16x8, b), c, 0, 0, 0); }
DI f32x4 mm16(u32x4 a, u32x4 b, f32x4 c) { return __builtin_amdgcn_mfma_f32_16x16x32_bf16(__builtin_bit_cast(bf16x8, a), __builtin_bit_cast(bf16x8, b), c, 0, 0, 0); }
DI u32 pk(float lo, float hi) { f32x2 v = {lo, hi}; return __builtin_bit_cast(u32, __builtin_convertvector(v, bf16x2)); }
DI u32x4 pk8(const f32x16& a, int s) { return u32x4{pk(a[8 * s], a[8 * s + 1]), pk(a[8 * s + 2], a[8 * s + 3]), pk(a[8 * s + 4], a[8 * s + 5]), pk(a[8 * s + 6], a[8 * s + 7])}; }
DI float f_(u32 x) { return __builtin_bit_cast(float, x); }
DI u32 u_(float x) { return __builtin_bit_cast(u32, x); }
DI float xmax32(float x) { auto r = __builtin_amdgcn_permlane32_swap(u_(x), u_(x), false, false); return fmaxf(f_(r[0]), f_(r[1])); }
DI float xmin32(float x) { auto r = __builtin_amdgcn_permlane32_swap(u_(x), u_(x), false, false); return fminf(f_(r[0]), f_(r[1])); }
DI float xadd32(float x) { auto r = __builtin_amdgcn_permlane32_swap(u_(x), u_(x), false, false); return f_(r[0]) + f_(r[1]); }
DI u32x4 gld(rsrc_t rs, u32 off) { return __builtin_amdgcn_raw_buffer_load_b128(rs, off, 0, SC1); }
DI void gst(rsrc_t rs, u32 off, u32x4 v) { __builtin_amdgcn_raw_buffer_store_b128(v, rs, off, 0, SC1); }
DI void gst64(rsrc_t rs, u32 off, u32x2 v) { __builtin_amdgcn_raw_buffer_store_b64(v, rs, off, 0, SC1); }
DI u32 opq(u32 x) { asm("" : "+v"(x)); return x; }
DI float sel(u32 m, float a, float b) { return f_((u_(a) & m) | (u_(b) & ~m)); }
DI void nap() { asm volatile("s_sleep 1" ::: "memory"); }
DI bool none(bool bad) { return __builtin_amdgcn_ballot_w64(bad) == 0; }
DI rsrc_t mk(const void* p, u32 bytes) { return __builtin_amdgcn_make_buffer_rsrc((void*)p, 0, bytes, 0x00020000); }
DI void wait_flags(rsrc_t rs, u32 off, u32x2 tag) {
#pragma nounroll
    for (int it = 0; it < SPIN; ++it) {
        u32x2 v = __builtin_amdgcn_raw_buffer_load_b64(rs, off, 0, SC1);
        if (none(off != OOB && (v.x != tag.x || v.y != tag.y))) break;
        nap();
    }
    asm volatile("" ::: "memory");
}

DI void phi(const f32x16& p, float keep, u32x4 (&o)[4]) {
    float mx = fmaxf(p[0], p[1]), mn = fminf(p[0], p[1]);
#pragma unroll
    for (int i = 2; i < 16; i += 2) { mx = fmaxf(fmaxf(mx, p[i]), p[i + 1]); mn = fminf(fminf(mn, p[i]), p[i + 1]); }
    mx = xmax32(mx) * -LOG2E; mn = xmin32(mn) * LOG2E;
    f32x16 a, b;
#pragma unroll
    for (int i = 0; i < 16; ++i) { a[i] = __builtin_amdgcn_exp2f(fmaf(p[i], LOG2E, mx)); b[i] = __builtin_amdgcn_exp2f(fmaf(p[i], -LOG2E, mn)); }
    float sa = ((a[12] + a[13]) + (a[14] + a[15])) * keep, sb = ((b[12] + b[13]) + (b[14] + b[15])) * keep;
#pragma unroll
    for (int i = 0; i < 12; i += 4) { sa += (a[i] + a[i + 1]) + (a[i + 2] + a[i + 3]); sb += (b[i] + b[i + 1]) + (b[i + 2] + b[i + 3]); }
    sa = __builtin_amdgcn_rcpf(xadd32(sa)); sb = __builtin_amdgcn_rcpf(xadd32(sb));
    a *= sa; b *= sb;
    o[0] = pk8(a, 0); o[1] = pk8(a, 1); o[2] = pk8(b, 0); o[3] = pk8(b, 1);
}

#define STAMP(i) do { if ((i) == 0) es0 = (u32)__builtin_amdgcn_s_memrealtime(); if ((i) == ESEL) es1 = (u32)__builtin_amdgcn_s_memrealtime(); } while (0)

struct Args { const void *x, *wa, *wb, *wphi, *gate; void* ws; const void *woa, *wob; void* out; const void* pad; };
__global__ __launch_bounds__(512, 2) void fused(const Args ar) {
    __shared__ __attribute__((aligned(1024))) char smem[163840];
    u32 es0 = 0, es1 = 0;
    STAMP(0);
    const u32 tid = threadIdx.x, l = tid & 63;
    const u32 w = __builtin_amdgcn_readfirstlane(tid >> 6);
    const u32 lb = (u32)(size_t)(LDSP(char))smem;
    {
        u64 pc; u32 len;
        asm volatile("s_getpc_b64 %0\n.Lfm_begin:\n\ts_mov_b32 %1, .Lfm_end-.Lfm_begin\n\ts_max_i32 %1, %1, 0\n\ts_nop 4" : "=s"(pc), "=s"(len)::"scc");
        dma(lb + L_JUNK + w * 1024, tid * 128, mk((const void*)pc, len), 0);
    }
    const void *gx = ar.x, *gwa = ar.wa, *gwb = ar.wb, *gwphi = ar.wphi, *ggate = ar.gate, *gwoa = ar.woa, *gwob = ar.wob;
    void *gws = ar.ws, *gout = ar.out;
    asm volatile("" : "+s"(gx), "+s"(gwa), "+s"(gwb), "+s"(gwphi), "+s"(ggate), "+s"(gws), "+s"(gwoa), "+s"(gwob), "+s"(gout));
    const u64 clk0 = __builtin_amdgcn_s_memtime();
    const u32 b = blockIdx.x, xcd = b & 7, bi = b >> 3;
    const bool isA = bi >= 16;
    const u32 t = (xcd >> 1) * 4 + ((bi & 15) >> 2), j = (xcd & 1) * 4 + (bi & 3);
    const u32 rg = w & 3, hh = w >> 2, head = 2 * j + hh;
    const u32 tok = l & 31, h = l >> 5, p = l & 15, g = l >> 4;
    const rsrc_t rx = mk(gx, 2048 * 896), rw = mk(isA ? gwa : gwb, 448 * 2688), rs = mk(gws, WS_BYTES);

    const u32 vx0 = (t * 128 + w * 16 + (l >> 3)) * 896 + (((l & 7) ^ g) << 4);
    const u32 vx1 = (vx0 + 8 * 896) ^ 64;
    const u32 wsrc = (l & 7) ^ ((g & 1) << 2);
    const u32 vw = wsrc == 7 ? OOB : (w * 8 + (l >> 3)) * 2688 + j * 112 + wsrc * 16;
    auto chunk1 = [&](u32 c, int q) {
        const u32 sl = lb + (c & 3) * 32768;
        if (q == 0) dma(sl + w * 2048, vx0, rx, c * 128);
        if (q == 1) dma(sl + w * 2048 + 1024, vx1, rx, c * 128);
        if (q == 2) dma(sl + 16384 + w * 1024, vw, rw, c * (64 * 2688) + 896);
        if (q == 3) dma(sl + 24576 + w * 1024, vw, rw, c * (64 * 2688) + 1792);
    };
    auto chunk = [&](u32 c) { chunk1(c, 0); chunk1(c, 1); chunk1(c, 2); chunk1(c, 3); };
    if (isA) {
        const u32 n = ((bi & 15) * 8 + w) * 64 + l, jb = (xcd & 1) * 3;
        const u32 n1 = n - 3584, n2 = n - 5376;
        dma4(lb + L_JUNK + 8192 + w * 256, n < 3584 ? (t & 12) * (128 * 896) + n * 128 : OOB, rx);
        const rsrc_t rwb = mk(gwb, 448 * 2688);
        dma4(lb + L_JUNK + 8192 + w * 256, n1 < 3584 ? (n1 >> 3) * 2688 + (((n1 >> 2) & 1) * 7 + 7 + jb + (n1 & 3)) * 128 : OOB, rwb);
        dma4(lb + L_JUNK + 8192 + w * 256, n < 5376 ? (n >> 2) * 896 + (jb + (n & 3)) * 128 : OOB, rw);
        dma4(lb + L_JUNK + 8192 + w * 256, n2 < 1792 ? (n2 >> 2) * 2688 + (jb + (n2 & 3)) * 128 : OOB, rwb);
    }
    chunk(0);
    u32 gatev; u32x2 e0v;
    asm volatile("buffer_load_dwordx2 %0, off, %1, 0 sc1" : "=v"(e0v) : "s"(rs) : "memory");
    asm volatile("buffer_load_ushort %0, %1, %2, 0 offen" : "=v"(gatev) : "v"(tid * 2), "s"(mk(ggate, 896)) : "memory");
    {
        const rsrc_t rp = mk(gwphi, isA ? 0 : 16 * 1568);
        dma(lb + L_WPHI + w * 2048, l * 16, rp, head * 1568);
        dma(lb + L_WPHI + w * 2048 + 1024, l < 34 ? l * 16 + 1024 : OOB, rp, head * 1568);
    }
    chunk(1);
    if (PRO == 3) chunk(2);
    STAMP(13);

    const u32 xa = lb + (rg * 32 + tok) * 128 + ((h ^ ((tok >> 1) & 7)) << 4);
    const u32 wa = lb + (8 * h + (p >> 2)) * 128 + ((56 * hh + 32 * (g & 1) + 8 * (p & 3)) ^ (((p >> 3) & 1) << 6));

    const u32 xah = opq(xa + 65536), wah = opq(wa + 65536);
    u32x4 xf[28], wphi[2];
    f32x16 ak = {}, av = {};
    u32x4 wk[4], wv[4];
    auto rd = [&](int c, int s) {
        const u32 sl = (c & 1) * 32768, xb = (c & 2) ? xah : xa, wb = (c & 2) ? wah : wa;
        xf[4 * c + s] = r128((xb ^ (32 * s)) + sl);
        wk[s] = rtr2(wb + sl + 16384 + s * 2048, wb + sl + 16384 + s * 2048 + 512);
        wv[s] = rtr2(wb + sl + 24576 + s * 2048, wb + sl + 24576 + s * 2048 + 512);
    };
    auto mm = [&](int c, int s) { ak = mm32(wk[s], xf[4 * c + s], ak); av = mm32(xf[4 * c + s], wv[s], av); };
    auto wq_dma = [&](u32 q) { dma(lb + L_WQ + q * 8192 + w * 1024, vw, rw, q * (64 * 2688)); };
    auto mk_ = [&](int c, int s) { ak = mm32(wk[s], xf[4 * c + s], ak); };
    auto mv_ = [&](int c, int s) { av = mm32(xf[4 * c + s], wv[s], av); };
    auto issue = [&](int c, int q) {
        if (c + PRO < 7) chunk1(c + PRO, q);
        else if (c + PRO == 7) wq_dma(3 + q);
        else if (c + PRO == 8 && q < 3) wq_dma(q);
        SCHED();
    };
    auto iter = [&](auto cc) {
        constexpr int c = decltype(cc)::value;
        constexpr int issued = 4 * (PRO + c) + 4 - (PRO + c > 8 ? 1 : 0) - (PRO + c > 9 ? 4 * (PRO + c - 9) : 0);
        constexpr int upto = 4 * (c + 1) + (c > 0 ? 4 : 0);
        waitbar<issued - upto>();
        STAMP(16 + c);
        rd(c, 0); rd(c, 1);
        if (c == 1) {
            const u32 fq = 4 * (g & 1) + (p & 3);
            const u32 pa = lb + L_WPHI + w * 2048 + (4 * h + (p >> 2)) * 56 + (fq == 7 ? 6 : fq) * 8;
            wphi[0] = rtr2(pa, pa + 8 * 56); wphi[1] = rtr2(pa + 16 * 56, pa + 24 * 56);
        }
        SCHED();
        if (c == ADC && isA) {
#pragma nounroll
            for (int it = 0; it < 256 && __builtin_amdgcn_s_memtime() - clk0 < ADELAY; ++it) nap();
        }
        if (c > 0) { mk_(c - 1, 2); mv_(c - 1, 2); SCHED(); }
        issue(c, 0);
        if (c > 0) { mk_(c - 1, 3); mv_(c - 1, 3); SCHED(); }
        issue(c, 1);
        rd(c, 2); rd(c, 3);
        SCHED();
        mm(c, 0); SCHED(); issue(c, 2);
        mm(c, 1); SCHED(); issue(c, 3);
    };
    iter(ic<0>{}); iter(ic<1>{}); iter(ic<2>{}); iter(ic<3>{}); iter(ic<4>{}); iter(ic<5>{}); iter(ic<6>{});
    mm(6, 2); mm(6, 3);
    asm volatile("" : "+v"(gatev), "+v"(e0v));
    const u64 tg = (((u64)(u32)__builtin_amdgcn_readfirstlane(e0v.y) << 32) | (u32)__builtin_amdgcn_readfirstlane(e0v.x)) + 0x9E3779B97F4A7C15ull;
    const u32x2 tag = {(u32)tg, (u32)(tg >> 32)};
    STAMP(2);

    const u32 i2 = bi;
    const bool p2 = i2 < 28;
    const u32 u2 = xcd * 4 + i2 / 7, cb = i2 % 7;
    const u32 vwo = (w * 8 + (l >> 3)) * 896 + cb * 128 + (((l & 7) ^ (g << 1)) << 4);
    const u32 flo = l < 16 ? WS_FL + ((u2 >> 1) * 16 + l) * 8 : OOB;
    bool flags_ok = false;
    auto wo_dma = [&](const void* gp, u32 dst) {
        if (p2) {
            const rsrc_t ro = mk(gp, 448 * 896);
#pragma unroll
            for (u32 q = 0; q < 7; ++q) dma(lb + dst + q * 8192 + w * 1024, vwo, ro, q * (64 * 896));
        }
    };
    auto stage2 = [&]() {
        f32x16 aq = {};
        const u32 wq = opq(wa + L_WQ);
#pragma unroll
        for (int ks = 0; ks < 28; ++ks) aq = mm32(rtr2(wq + ks * 2048, wq + ks * 2048 + 512), xf[ks], aq);
        return aq;
    };
    const u32 ooff = ((t * 8 + rg * 2 + (tok >> 4)) * 16 + head) * 1024 + h * 256 + p * 16;
    auto publish_o = [&](f32x16 o, float r, u32 base) {
        o *= r;
        u32x4 o0 = pk8(o, 0), o1 = pk8(o, 1);
        if (h) { o1.z = 0; o1.w = 0; }
        gst(rs, base + ooff, o0); gst(rs, base + ooff + 512, o1);
    };
    const u32x4 kf0 = pk8(ak, 0), kf1 = pk8(ak, 1);
    u32x4 vf0 = pk8(av, 0), vf1 = pk8(av, 1);

    if (!isA) {
        const float keep = h ? 0.f : 1.f;
        {
            f32x16 pp = mm32(wphi[0], kf0, f32x16{});
            pp = mm32(wphi[1], kf1, pp);
            if (tok == 28) { vf0 = u32x4{0x3F803F80u, 0x3F803F80u, 0x3F803F80u, 0x3F803F80u}; vf1 = vf0; }
            w128(lb + L_VP + w * 2048 + l * 16, vf0); w128(lb + L_VP + w * 2048 + 1024 + l * 16, vf1);
            u32x4 ph[4];
            phi(pp, keep, ph);
            const u32 F = (((tok >> 1) & 3) << 2) | (((tok >> 3) & 1) << 1) | (tok & 1);
            const u32 pw = lb + L_PHI + w * 4096 + tok * 128 + ((h ^ F) << 3);
#pragma unroll
            for (int s = 0; s < 4; ++s) {
                const u32 cu = 8 * (s >> 1) + 2 * (s & 1);
                w64(pw ^ (cu << 3), u32x2{ph[s].x, ph[s].y});
                w64(pw ^ ((cu + 4) << 3), u32x2{ph[s].z, ph[s].w});
            }
        }
        LBAR();
        STAMP(3);
        {
            const u32 sign = rg >> 1, dt = rg & 1;
            const u32 F = ((2 * (g & 1) + (p >> 3)) << 2) | ((p >> 2) & 1);
            const u32 pr = lb + L_PHI + hh * 16384 + (16 * (g >> 1) + 4 * (g & 1) + (p >> 2)) * 128 + (((8 * sign + (p & 3)) ^ F) << 3);
            const u32 vr = lb + L_VP + hh * 8192 + g * 512 + (16 * dt + p) * 16;
            f32x4 c0 = {}, c1 = {};
#pragma unroll
            for (int r = 0; r < 4; ++r) {
                const u32x4 vv = r128(vr + r * 2048);
                c0 = mm16(rtr2(pr + r * 4096, (pr ^ 16) + r * 4096 + 1024), vv, c0);
                c1 = mm16(rtr2((pr ^ 32) + r * 4096, (pr ^ 48) + r * 4096 + 1024), vv, c1);
            }
            const u32 t0 = tag.x & 0xFFF, t1 = (tag.x >> 12) & 0xFFF, t2 = tag.y & 0xFFF, t3 = (tag.y >> 12) & 0xFFF;
            const u32 po = WS_P + (j * 8 + w) * 32768 + t * 1024 + l * 16;
#define TG(x, tt) (((u_(x) + 0x800u) & 0xFFFFF000u) | (tt))
            gst(rs, po, u32x4{TG(c0[0], t0), TG(c0[1], t1), TG(c0[2], t2), TG(c0[3], t3)});
            gst(rs, po + 16384, u32x4{TG(c1[0], t0), TG(c1[1], t1), TG(c1[2], t2), TG(c1[3], t3)});
        }
        STAMP(4);
        WAITBAR(2);
        STAMP(14);
        const u32x4 ones = {~0u, ~0u, ~0u, ~0u};
        gst(rs, WS_OB + ooff, ones); gst(rs, WS_OB + ooff + 512, ones);
        wo_dma(gwoa, L_WOA);
        const f32x16 aq = stage2();
        SCHED();
        STAMP(5);
        {
            const u32 un = t >> 1, cg = (t & 1) * 8 + w;
            const u32 po = WS_P + (j * 8 + un) * 32768 + (l >> 2) * 1024 + (4 * cg + (l & 3)) * 16;
            u32x4 a, bq;
        #pragma nounroll
    for (int it = 0; it < SPIN; ++it) {
                a = gld(rs, po); bq = gld(rs, po + 16384);
                const u32 bad = ((a.x ^ tag.x) | (bq.x ^ tag.x)) | ((a.y ^ (tag.x >> 12)) | (bq.y ^ (tag.x >> 12))) |
                                ((a.z ^ tag.y) | (bq.z ^ tag.y)) | ((a.w ^ (tag.y >> 12)) | (bq.w ^ (tag.y >> 12)));
                if (none((bad & 0xFFF) != 0)) break;
                nap();
            }
            STAMP(6);
            const u32 gq = cg >> 2;
            const u32 mb = gq == 3 ? 0u : 0xFFFFF000u;
            float uu[4];
#pragma unroll
            for (int i = 0; i < 4; ++i) {
                auto r = __builtin_amdgcn_permlane32_swap(a[i] & 0xFFFFF000u, bq[i] & mb, false, false);
                uu[i] = f_(r[0]) + f_(r[1]);
            }
            float ww[2];
#pragma unroll
            for (int i = 0; i < 2; ++i) {
                auto r = __builtin_amdgcn_permlane16_swap(u_(uu[i]), u_(uu[i + 2]), false, false);
                float y = f_(r[0]) + f_(r[1]);
                y += f_(__builtin_amdgcn_update_dpp(0, u_(y), 0x128, 0xf, 0xf, false));
                y += f_(__builtin_amdgcn_update_dpp(0, u_(y), 0x124, 0xf, 0xf, false));
                ww[i] = y;
            }
            const u32 pp = pk(ww[0], ww[1]), t16 = tag.x & 0xFFFF;
            const u32 s = ((un >> 1) & 1) * 2 + (gq >> 1);
            const u32 l32 = 16 * (un & 1) + ((4 * cg + (l & 3)) & 15) + 32 * (gq & 1);
            const u32 ko = WS_KVR + (((2 * j + (un >> 2)) * 4 + s) * 2 + (g >> 1)) * 1024 + l32 * 16 + (g & 1) * 8;
            gst64(rs, (l & 12) == 0 ? ko : OOB, u32x2{(pp << 16) | t16, (pp & 0xFFFF0000u) | t16});
        }
        WAITBAR(1);
        gst64(rs, tid == 0 ? WS_FL + (t * 16 + 8 + j) * 8 : OOB, tag);
        wo_dma(gwob, L_WOB);
        u32x4 pq[4];
        {
            f32x16 pp = mm32(wphi[0], pk8(aq, 0), f32x16{});
            pp = mm32(wphi[1], pk8(aq, 1), pp);
            phi(pp, keep, pq);
            asm volatile("" : "+v"(pq[0]), "+v"(pq[1]), "+v"(pq[2]), "+v"(pq[3]));
        }
        STAMP(15);
        {
            const u32 ko = WS_KVR + (head * 4 + rg) * 2048 + l * 16;
            u32x4 a, bq;
#pragma nounroll
            for (int it = 0; it < SPIN; ++it) {
                a = gld(rs, ko); bq = gld(rs, ko + 1024);
                const u32x2 fv = __builtin_amdgcn_raw_buffer_load_b64(rs, flo, 0, SC1);
                const u32 bad = ((a.x ^ tag.x) | (bq.x ^ tag.x)) | ((a.y ^ tag.x) | (bq.y ^ tag.x)) | ((a.z ^ tag.x) | (bq.z ^ tag.x)) | ((a.w ^ tag.x) | (bq.w ^ tag.x));
                flags_ok = none(l < 16 && (fv.x != tag.x || fv.y != tag.y));
                if (none((bad & 0xFFFF) != 0)) break;
                nap();
            }
            STAMP(7);
#define HI2(x, y) __builtin_amdgcn_perm(y, x, 0x07060302u)
            w128(lb + L_KVL + w * 1024 + l * 16, u32x4{HI2(a.x, a.y), HI2(a.z, a.w), HI2(bq.x, bq.y), HI2(bq.z, bq.w)});
            LBAR();
            f32x16 o = {};
#pragma unroll
            for (int s = 0; s < 4; ++s) o = mm32(r128(lb + L_KVL + (hh * 4 + s) * 1024 + l * 16), pq[s], o);
            auto r = __builtin_amdgcn_permlane32_swap(u_(o[12]), u_(o[12]), false, false);
            publish_o(o, __builtin_amdgcn_rcpf(f_(r[1]) + 1e-6f), WS_OB);
        }
        STAMP(8);
    } else {
        const u32 kxo = WS_KX + (t * 16 + head) * 3072 + l * 16;
        if (rg < 3) {
            const u32 ax = lb + L_AX + w * 3072 + l * 16;
            w128(ax, kf0); w128(ax + 1024, kf1); w128(ax + 2048, vf1);
        } else {
            gst(rs, kxo, kf0); gst(rs, kxo + 1024, kf1); gst(rs, kxo + 2048, vf1);
            WAITVM(0);
            gst64(rs, l == 0 ? WS_KF + (t * 16 + head) * 8 : OOB, tag);
        }
        WAITBAR(0);
        STAMP(3);
        if (j == 0) {
            if (tid < 448) w128(lb + L_WQ + tid * 128 + ((7 ^ ((tid & 2) << 1)) << 4), u32x4{gatev, 0, 0, 0});
            LBAR();
        }
        wo_dma(gwoa, L_WOA);
        const f32x16 aq = stage2();
        STAMP(4);
        if (j == 0 && hh == 1) {
            const float gg = __builtin_amdgcn_rcpf(1.f + __builtin_amdgcn_exp2f(aq[12] * -LOG2E));
            __builtin_amdgcn_raw_buffer_store_b32(u_(gg), rs, h ? WS_G + (t * 128 + rg * 32 + tok) * 4 : OOB, 0, SC1);
        }
        u32x4 qf0 = pk8(aq, 0), qf1 = pk8(aq, 1);
        if (h) { qf1.z = 0; qf1.w = 0; }
        u32x4 kp0, kp1, vp;
        bool hasprev = true;
        if (rg > 0) {
            const u32 ax = lb + L_AX + (w - 1) * 3072 + l * 16;
            kp0 = r128(ax); kp1 = r128(ax + 1024); vp = r128(ax + 2048);
        } else if (t > 0) {
            wait_flags(rs, l == 0 ? WS_KF + ((t - 1) * 16 + head) * 8 : OOB, tag);
            kp0 = gld(rs, kxo - 16 * 3072); kp1 = gld(rs, kxo - 16 * 3072 + 1024); vp = gld(rs, kxo - 16 * 3072 + 2048);
        } else {
            kp0 = u32x4{}; kp1 = u32x4{}; vp = u32x4{}; hasprev = false;
        }
        STAMP(5);
        f32x16 so = mm32(kf0, qf0, f32x16{}); so = mm32(kf1, qf1, so);
        f32x16 sp = mm32(kp0, qf0, f32x16{}); sp = mm32(kp1, qf1, sp);
        const u32 om = opq(0u - (tok & 1));
        const u32 base = (tok & ~1u) - 4 * h, basep = hasprev ? base + 32 : 1000u;
        float sv[12];
        const float NEG = -1e30f;
#pragma unroll
        for (int i = 0; i < 8; ++i) { const float v = sel(om, so[2 * i + 1], so[2 * i]); sv[i] = (base - (8 * (i >> 1) + 2 * (i & 1))) <= 14u ? v : NEG; }
#pragma unroll
        for (int i = 4; i < 8; ++i) { const float v = sel(om, sp[2 * i + 1], sp[2 * i]); sv[4 + i] = (basep - (8 * (i >> 1) + 2 * (i & 1))) <= 14u ? v : NEG; }
        float mx = fmaxf(sv[0], sv[1]);
#pragma unroll
        for (int i = 2; i < 12; i += 2) mx = fmaxf(fmaxf(mx, sv[i]), sv[i + 1]);
        const float sc = 0.18898223650461362f * LOG2E;
        mx = xmax32(mx) * -sc;
        float sum = 0.f;
        u32 pd[12];
        const u32 pm = om ^ 0x0000FFFFu;
#pragma unroll
        for (int i = 0; i < 12; ++i) { const float e = __builtin_amdgcn_exp2f(fmaf(sv[i], sc, mx)); sum += e; pd[i] = pk(e, e) & pm; }
        f32x16 o = mm32(vf0, u32x4{pd[0], pd[1], pd[2], pd[3]}, f32x16{});
        o = mm32(vf1, u32x4{pd[4], pd[5], pd[6], pd[7]}, o);
        o = mm32(vp, u32x4{pd[8], pd[9], pd[10], pd[11]}, o);
        publish_o(o, __builtin_amdgcn_rcpf(xadd32(sum)), WS_OA);
        STAMP(6);
        WAITBAR(0);
        gst64(rs, tid == 0 ? WS_FL + (t * 16 + j) * 8 : OOB, tag);
        wo_dma(gwob, L_WOB);
        STAMP(8);
    }

    if (p2) {
        const u32 tt = w & 3, kh = w >> 2;
        if (!flags_ok) wait_flags(rs, flo, tag);
        if (isA) WAITBAR(0);
        STAMP(9);
        const u32 oo = ((u2 * 4 + tt) * 16 + kh * 8) * 1024 + l * 16;
        u32x4 of[8];
#pragma unroll
        for (int e = 0; e < 8; ++e) of[e] = gld(rs, WS_OA + oo + e * 1024);
        const float gg = f_(__builtin_amdgcn_raw_buffer_load_b32(rs, WS_G + (u2 * 64 + tt * 16 + p) * 4, 0, SC1));
        const u32 f0 = 2 * (g & 1) + (p >> 3);
        const u32 wb0 = lb + (224 * kh + 16 * (g >> 1) + 4 * (g & 1) + (p >> 2)) * 128 + ((((p & 3) >> 1) ^ (2 * f0)) << 4) + (p & 1) * 8;
        const u32 wb1 = wb0 + (g == 3 ? 0 : 1024);
        u32 wr0[4], wr1[4];
        auto wbase = [&](u32 img) {
#pragma unroll
            for (int x = 0; x < 4; ++x) { wr0[x] = opq((wb0 + img) ^ (x << 5)); wr1[x] = opq((wb1 + img) ^ (x << 5)); }
        };
        auto wfrag = [&](int e, int nt) {
            const int x = nt ^ (2 * (e & 1));
            return rtr2(wr0[x] + e * 3584, wr1[x] + e * 3584);
        };
        wbase(L_WOA);
        f32x4 ca[4] = {}, cbb[4] = {};
        u32x4 ob[8];
#pragma unroll
        for (int e = 0; e < 8; ++e) {
#pragma unroll
            for (int nt = 0; nt < 4; ++nt) ca[nt] = mm16(wfrag(e, nt), of[e], ca[nt]);
            if (e == 3) {
#pragma unroll
                for (int q = 0; q < 8; ++q) ob[q] = gld(rs, WS_OB + oo + q * 1024);
            }
        }
        STAMP(10);
        u32x4 wf[8][4];
        wbase(L_WOB);
#pragma unroll
        for (int e = 0; e < 8; ++e)
#pragma unroll
            for (int nt = 0; nt < 4; ++nt) wf[e][nt] = wfrag(e, nt);
#pragma nounroll
        for (int it = 0; it < SPIN; ++it) {
            u32 m = 0;
#pragma unroll
            for (int e = 0; e < 8; ++e) m = max(max(m, max(ob[e].x, ob[e].y)), max(ob[e].z, ob[e].w));
            if (none(m == ~0u)) break;
#pragma nounroll
            for (; it < SPIN; ++it) {
                const u32x4 v = gld(rs, WS_OB + oo + 7 * 1024);
                if (none(max(max(v.x, v.y), max(v.z, v.w)) == ~0u)) break;
                nap();
            }
#pragma unroll
            for (int e = 0; e < 8; ++e) ob[e] = gld(rs, WS_OB + oo + e * 1024);
        }
        STAMP(11);
#pragma unroll
        for (int e = 0; e < 8; ++e)
#pragma unroll
            for (int nt = 0; nt < 4; ++nt) cbb[nt] = mm16(wf[e][nt], ob[e], cbb[nt]);
        const u32 xo = lb + L_XO + kh * 16384 + (tt * 16 + p) * 256 + ((g ^ (p & 7)) << 4);
#pragma unroll
        for (int nt = 0; nt < 4; ++nt) {
            const f32x4 r = ca[nt] * gg + cbb[nt] * (1.f - gg);
            w128(xo ^ (nt << 6), __builtin_bit_cast(u32x4, r));
        }
        LBAR();
        const u32 tk = tid >> 3, c8 = tid & 7;
        const u32 xr = lb + L_XO + tk * 256 + (((2 * c8) ^ (tk & 7)) << 4);
        const f32x4 r0 = __builtin_bit_cast(f32x4, r128(xr)) + __builtin_bit_cast(f32x4, r128(xr + 16384));
        const f32x4 r1 = __builtin_bit_cast(f32x4, r128(xr ^ 16)) + __builtin_bit_cast(f32x4, r128((xr ^ 16) + 16384));
        __builtin_amdgcn_raw_buffer_store_b128(u32x4{pk(r0[0], r0[1]), pk(r0[2], r0[3]), pk(r1[0], r1[1]), pk(r1[2], r1[3])},
                                               mk(gout, 2048 * 896), (u2 * 64 + tk) * 896 + cb * 128 + c8 * 16, 0, 0);
        STAMP(12);
    } else if (b == 255 && w < 4) {
        wait_flags(rs, WS_FL + tid * 8, tag);
        asm volatile("s_barrier" ::: "memory");
        gst64(rs, tid == 0 ? WS_E0 : OOB, tag);
    } else if (b == 255) {
        asm volatile("s_barrier" ::: "memory");
    }
    {
        if (ESEL == 99) es1 = (u32)__builtin_amdgcn_s_memrealtime();
        if (EONLYB && isA) es1 = 0;
        const rsrc_t re = mk((char*)gws + (16u << 20), 65536);
        gst(re, l == 0 ? (b * 8 + w) * 16 : OOB, u32x4{es0, es1, tag.x, tag.y});
        if (b == 255 && w == 0) {
            u32 mn = 0x7fffffffu, mx = 0;
            for (int it = 0; it < SPIN; ++it) {
                u32x4 v[32]; bool bad = false;
#pragma unroll
                for (int q = 0; q < 32; ++q) v[q] = gld(re, (q * 64 + l) * 16);
                mn = 0x7fffffffu; mx = 0;
#pragma unroll
                for (int q = 0; q < 32; ++q) {
                    bad |= v[q].z != tag.x || v[q].w != tag.y;
                    mn = min(mn, v[q].x - es0 + 0x100000u);
                    if (v[q].y) mx = max(mx, v[q].y - es0 + 0x100000u);
                }
                if (none(bad)) break;
                nap();
            }
            for (int o = 1; o < 64; o <<= 1) { mn = min(mn, (u32)__shfl_xor((int)mn, o)); mx = max(mx, (u32)__shfl_xor((int)mx, o)); }
            u32 Q = (mx - mn) * EK; if (Q > 10000) Q = 10000;
            const u32 t0 = (u32)__builtin_amdgcn_s_memrealtime();
            while ((u32)__builtin_amdgcn_s_memrealtime() - t0 < Q) nap();
        }
    }
    asm volatile(".Lfm_end:" ::: "memory");
}

extern "C" void kernel_launch(void* const* d_in, const int* in_sizes, int n_in, void* d_out, int out_size, void* d_ws, size_t ws_size, hipStream_t stream) {
    (void)in_sizes; (void)n_in; (void)out_size; (void)ws_size;
    Args a;
    a.x = d_in[0]; a.wa = d_in[1]; a.wb = d_in[4]; a.wphi = d_in[5]; a.gate = d_in[7]; a.ws = d_ws; a.woa = d_in[2]; a.wob = d_in[6]; a.out = d_out; a.pad = nullptr;
    hipLaunchKernelGGL(fused, dim3(256), dim3(512), 0, stream, a);
}
```

```cpp
#define OUTAUX 16
#include <hip/hip_runtime.h>
#include <stdint.h>

typedef unsigned u32;
typedef unsigned long long u64;
typedef u32 u32x4 __attribute__((ext_vector_type(4)));
typedef u32 u32x2 __attribute__((ext_vector_type(2)));
typedef float f32x2 __attribute__((ext_vector_type(2)));
typedef float f32x4 __attribute__((ext_vector_type(4)));
typedef float f32x16 __attribute__((ext_vector_type(16)));
typedef __bf16 bf16x8 __attribute__((ext_vector_type(8)));
typedef __bf16 bf16x2 __attribute__((ext_vector_type(2)));
typedef short s16x4 __attribute__((ext_vector_type(4)));
typedef __amdgpu_buffer_rsrc_t rsrc_t;
#define LDSP(T) __attribute__((address_space(3))) T*
#define DI __device__ __forceinline__

constexpr u32 WS_E0 = 0;
constexpr u32 WS_FL = 256;
constexpr u32 WS_KF = 4096;
constexpr u32 WS_G = 8192;
constexpr u32 WS_KVR = 16384;
constexpr u32 WS_KX = 256 * 1024;
constexpr u32 WS_P = 1024 * 1024;
constexpr u32 WS_OA = 3 * 1024 * 1024;
constexpr u32 WS_OB = 5 * 1024 * 1024;
constexpr u32 WS_BYTES = 7 * 1024 * 1024;
constexpr u32 L_WQ = 104 * 1024, L_WOA = 48 * 1024, L_WOB = 104 * 1024;
constexpr u32 L_PHI = 0, L_VP = 32 * 1024, L_WPHI = 128 * 1024, L_JUNK = 144 * 1024;
constexpr u32 L_AX = 0, L_KVL = 0, L_XO = 8 * 1024;
constexpr u32 OOB = 0x80000000u;
constexpr int SPIN = 1 << 15;
constexpr int SC1 = 16;
#define ADELAY 3500
#define ADC 0
#define PRO 2
#define WARM 1
template <int N> struct ic { static constexpr int value = N; };
constexpr float LOG2E = 1.4426950408889634f;

DI void dma(u32 m0v, u32 voff, rsrc_t rs, u32 soff) {
    asm volatile("s_mov_b32 m0, %0\n\ts_nop 0\n\tbuffer_load_dwordx4 %1, %2, %3 offen lds" ::"s"(m0v), "v"(voff), "s"(rs), "s"(soff) : "memory");
}
DI void dma4(u32 m0v, u32 voff, rsrc_t rs) {
    asm volatile("s_mov_b32 m0, %0\n\ts_nop 0\n\tbuffer_load_dword %1, %2, 0 offen lds" ::"s"(m0v), "v"(voff), "s"(rs) : "memory");
}
template <int N> DI void waitbar() { asm volatile("s_waitcnt vmcnt(%0) lgkmcnt(0)\n\ts_barrier" ::"i"(N) : "memory"); }
#define WAITBAR(N) asm volatile("s_waitcnt vmcnt(" #N ") lgkmcnt(0)\n\ts_barrier" ::: "memory")
#define LBAR() asm volatile("s_waitcnt lgkmcnt(0)\n\ts_barrier" ::: "memory")
#define WAITVM(N) asm volatile("s_waitcnt vmcnt(" #N ")" ::: "memory")
#define SCHED() __builtin_amdgcn_sched_barrier(0)
DI u32x4 r128(u32 a) { return *(LDSP(const u32x4))(size_t)a; }
DI void w128(u32 a, u32x4 v) { *(LDSP(u32x4))(size_t)a = v; }
DI void w64(u32 a, u32x2 v) { *(LDSP(u32x2))(size_t)a = v; }
DI u32x2 rtr(u32 a) { return __builtin_bit_cast(u32x2, __builtin_amdgcn_ds_read_tr16_b64_v4i16((LDSP(s16x4))(size_t)a)); }
DI u32x4 rtr2(u32 a, u32 b) { u32x2 x = rtr(a), y = rtr(b); return u32x4{x.x, x.y, y.x, y.y}; }
DI f32x16 mm32(u32x4 a, u32x4 b, f32x16 c) { return __builtin_amdgcn_mfma_f32_32x32x16_bf16(__builtin_bit_cast(bf16x8, a), __builtin_bit_cast(bf16x8, b), c, 0, 0, 0); }
DI f32x4 mm16(u32x4 a, u32x4 b, f32x4 c) { return __builtin_amdgcn_mfma_f32_16x16x32_bf16(__builtin_bit_cast(bf16x8, a), __builtin_bit_cast(bf16x8, b), c, 0, 0, 0); }
DI u32 pk(float lo, float hi) { f32x2 v = {lo, hi}; return __builtin_bit_cast(u32, __builtin_convertvector(v, bf16x2)); }
DI u32x4 pk8(const f32x16& a, int s) { return u32x4{pk(a[8 * s], a[8 * s + 1]), pk(a[8 * s + 2], a[8 * s + 3]), pk(a[8 * s + 4], a[8 * s + 5]), pk(a[8 * s + 6], a[8 * s + 7])}; }
DI float f_(u32 x) { return __builtin_bit_cast(float, x); }
DI u32 u_(float x) { return __builtin_bit_cast(u32, x); }
DI float xmax32(float x) { auto r = __builtin_amdgcn_permlane32_swap(u_(x), u_(x), false, false); return fmaxf(f_(r[0]), f_(r[1])); }
DI float xmin32(float x) { auto r = __builtin_amdgcn_permlane32_swap(u_(x), u_(x), false, false); return fminf(f_(r[0]), f_(r[1])); }
DI float xadd32(float x) { auto r = __builtin_amdgcn_permlane32_swap(u_(x), u_(x), false, false); return f_(r[0]) + f_(r[1]); }
DI u32x4 gld(rsrc_t rs, u32 off) { return __builtin_amdgcn_raw_buffer_load_b128(rs, off, 0, SC1); }
DI void gst(rsrc_t rs, u32 off, u32x4 v) { __builtin_amdgcn_raw_buffer_store_b128(v, rs, off, 0, SC1); }
DI void gst64(rsrc_t rs, u32 off, u32x2 v) { __builtin_amdgcn_raw_buffer_store_b64(v, rs, off, 0, SC1); }
DI u32 opq(u32 x) { asm("" : "+v"(x)); return x; }
DI float sel(u32 m, float a, float b) { return f_((u_(a) & m) | (u_(b) & ~m)); }
DI void nap() { asm volatile("s_sleep 1" ::: "memory"); }
DI bool none(bool bad) { return __builtin_amdgcn_ballot_w64(bad) == 0; }
DI rsrc_t mk(const void* p, u32 bytes) { return __builtin_amdgcn_make_buffer_rsrc((void*)p, 0, bytes, 0x00020000); }
DI void wait_flags(rsrc_t rs, u32 off, u32x2 tag) {
#pragma nounroll
    for (int it = 0; it < SPIN; ++it) {
        u32x2 v = __builtin_amdgcn_raw_buffer_load_b64(rs, off, 0, SC1);
        if (none(off != OOB && (v.x != tag.x || v.y != tag.y))) break;
        nap();
    }
    asm volatile("" ::: "memory");
}

DI void phi(const f32x16& p, float keep, u32x4 (&o)[4]) {
    float mx = fmaxf(p[0], p[1]), mn = fminf(p[0], p[1]);
#pragma unroll
    for (int i = 2; i < 16; i += 2) { mx = fmaxf(fmaxf(mx, p[i]), p[i + 1]); mn = fminf(fminf(mn, p[i]), p[i + 1]); }
    mx = xmax32(mx) * -LOG2E; mn = xmin32(mn) * LOG2E;
    f32x16 a, b;
#pragma unroll
    for (int i = 0; i < 16; ++i) { a[i] = __builtin_amdgcn_exp2f(fmaf(p[i], LOG2E, mx)); b[i] = __builtin_amdgcn_exp2f(fmaf(p[i], -LOG2E, mn)); }
    float sa = ((a[12] + a[13]) + (a[14] + a[15])) * keep, sb = ((b[12] + b[13]) + (b[14] + b[15])) * keep;
#pragma unroll
    for (int i = 0; i < 12; i += 4) { sa += (a[i] + a[i + 1]) + (a[i + 2] + a[i + 3]); sb += (b[i] + b[i + 1]) + (b[i + 2] + b[i + 3]); }
    sa = __builtin_amdgcn_rcpf(xadd32(sa)); sb = __builtin_amdgcn_rcpf(xadd32(sb));
    a *= sa; b *= sb;
    o[0] = pk8(a, 0); o[1] = pk8(a, 1); o[2] = pk8(b, 0); o[3] = pk8(b, 1);
}

#define STAMP(i)

struct Args { const void *x, *wa, *wb, *wphi, *gate; void* ws; const void *woa, *wob; void* out; const void* pad; };
__global__ __launch_bounds__(512, 2) void fused(const Args ar) {
    __shared__ __attribute__((aligned(1024))) char smem[163840];
    STAMP(0);
    const u32 tid = threadIdx.x, l = tid & 63;
    const u32 w = __builtin_amdgcn_readfirstlane(tid >> 6);
    const u32 lb = (u32)(size_t)(LDSP(char))smem;
    {
        u64 pc; u32 len;
        asm volatile("s_getpc_b64 %0\n.Lfm_begin:\n\ts_mov_b32 %1, .Lfm_end-.Lfm_begin\n\ts_max_i32 %1, %1, 0\n\ts_nop 4" : "=s"(pc), "=s"(len)::"scc");
        dma(lb + L_JUNK + w * 1024, tid * 128, mk((const void*)pc, len), 0);
    }
    const void *gx = ar.x, *gwa = ar.wa, *gwb = ar.wb, *gwphi = ar.wphi, *ggate = ar.gate, *gwoa = ar.woa, *gwob = ar.wob;
    void *gws = ar.ws, *gout = ar.out;
    asm volatile("" : "+s"(gx), "+s"(gwa), "+s"(gwb), "+s"(gwphi), "+s"(ggate), "+s"(gws), "+s"(gwoa), "+s"(gwob), "+s"(gout));
    const u64 clk0 = __builtin_amdgcn_s_memtime();
    const u32 b = blockIdx.x, xcd = b & 7, bi = b >> 3;
    const bool isA = bi >= 16;
    const u32 t = (xcd >> 1) * 4 + ((bi & 15) >> 2), j = (xcd & 1) * 4 + (bi & 3);
    const u32 rg = w & 3, hh = w >> 2, head = 2 * j + hh;
    const u32 tok = l & 31, h = l >> 5, p = l & 15, g = l >> 4;
    const rsrc_t rx = mk(gx, 2048 * 896), rw = mk(isA ? gwa : gwb, 448 * 2688), rs = mk(gws, WS_BYTES);

    const u32 vx0 = (t * 128 + w * 16 + (l >> 3)) * 896 + (((l & 7) ^ g) << 4);
    const u32 vx1 = (vx0 + 8 * 896) ^ 64;
    const u32 wsrc = (l & 7) ^ ((g & 1) << 2);
    const u32 vw = wsrc == 7 ? OOB : (w * 8 + (l >> 3)) * 2688 + j * 112 + wsrc * 16;
    auto chunk1 = [&](u32 c, int q) {
        const u32 sl = lb + (c & 3) * 32768;
        if (q == 0) dma(sl + w * 2048, vx0, rx, c * 128);
        if (q == 1) dma(sl + w * 2048 + 1024, vx1, rx, c * 128);
        if (q == 2) dma(sl + 16384 + w * 1024, vw, rw, c * (64 * 2688) + 896);
        if (q == 3) dma(sl + 24576 + w * 1024, vw, rw, c * (64 * 2688) + 1792);
    };
    auto chunk = [&](u32 c) { chunk1(c, 0); chunk1(c, 1); chunk1(c, 2); chunk1(c, 3); };
    if (isA) {
        const u32 n = ((bi & 15) * 8 + w) * 64 + l, jb = (xcd & 1) * 3;
        const u32 n1 = n - 3584, n2 = n - 5376;
        dma4(lb + L_JUNK + 8192 + w * 256, n < 3584 ? (t & 12) * (128 * 896) + n * 128 : OOB, rx);
        const rsrc_t rwb = mk(gwb, 448 * 2688);
        dma4(lb + L_JUNK + 8192 + w * 256, n1 < 3584 ? (n1 >> 3) * 2688 + (((n1 >> 2) & 1) * 7 + 7 + jb + (n1 & 3)) * 128 : OOB, rwb);
        dma4(lb + L_JUNK + 8192 + w * 256, n < 5376 ? (n >> 2) * 896 + (jb + (n & 3)) * 128 : OOB, rw);
        dma4(lb + L_JUNK + 8192 + w * 256, n2 < 1792 ? (n2 >> 2) * 2688 + (jb + (n2 & 3)) * 128 : OOB, rwb);
    }
    chunk(0);
    u32 gatev; u32x2 e0v;
    asm volatile("buffer_load_dwordx2 %0, off, %1, 0 sc1" : "=v"(e0v) : "s"(rs) : "memory");
    asm volatile("buffer_load_ushort %0, %1, %2, 0 offen" : "=v"(gatev) : "v"(tid * 2), "s"(mk(ggate, 896)) : "memory");
    {
        const rsrc_t rp = mk(gwphi, isA ? 0 : 16 * 1568);
        dma(lb + L_WPHI + w * 2048, l * 16, rp, head * 1568);
        dma(lb + L_WPHI + w * 2048 + 1024, l < 34 ? l * 16 + 1024 : OOB, rp, head * 1568);
    }
    chunk(1);
    if (PRO == 3) chunk(2);
    STAMP(13);

    const u32 xa = lb + (rg * 32 + tok) * 128 + ((h ^ ((tok >> 1) & 7)) << 4);
    const u32 wa = lb + (8 * h + (p >> 2)) * 128 + ((56 * hh + 32 * (g & 1) + 8 * (p & 3)) ^ (((p >> 3) & 1) << 6));

    const u32 xah = opq(xa + 65536), wah = opq(wa + 65536);
    u32x4 xf[28], wphi[2];
    f32x16 ak = {}, av = {};
    u32x4 wk[4], wv[4];
    auto rd = [&](int c, int s) {
        const u32 sl = (c & 1) * 32768, xb = (c & 2) ? xah : xa, wb = (c & 2) ? wah : wa;
        xf[4 * c + s] = r128((xb ^ (32 * s)) + sl);
        wk[s] = rtr2(wb + sl + 16384 + s * 2048, wb + sl + 16384 + s * 2048 + 512);
        wv[s] = rtr2(wb + sl + 24576 + s * 2048, wb + sl + 24576 + s * 2048 + 512);
    };
    auto mm = [&](int c, int s) { ak = mm32(wk[s], xf[4 * c + s], ak); av = mm32(xf[4 * c + s], wv[s], av); };
    auto wq_dma = [&](u32 q) { dma(lb + L_WQ + q * 8192 + w * 1024, vw, rw, q * (64 * 2688)); };
    auto mk_ = [&](int c, int s) { ak = mm32(wk[s], xf[4 * c + s], ak); };
    auto mv_ = [&](int c, int s) { av = mm32(xf[4 * c + s], wv[s], av); };
    auto issue = [&](int c, int q) {
        if (c + PRO < 7) chunk1(c + PRO, q);
        else if (c + PRO == 7) wq_dma(3 + q);
        else if (c + PRO == 8 && q < 3) wq_dma(q);
        SCHED();
    };
    auto iter = [&](auto cc) {
        constexpr int c = decltype(cc)::value;
        constexpr int issued = 4 * (PRO + c) + 4 - (PRO + c > 8 ? 1 : 0) - (PRO + c > 9 ? 4 * (PRO + c - 9) : 0);
        constexpr int upto = 4 * (c + 1) + (c > 0 ? 4 : 0);
        waitbar<issued - upto>();
        STAMP(16 + c);
        rd(c, 0); rd(c, 1);
        if (c == 1) {
            const u32 fq = 4 * (g & 1) + (p & 3);
            const u32 pa = lb + L_WPHI + w * 2048 + (4 * h + (p >> 2)) * 56 + (fq == 7 ? 6 : fq) * 8;
            wphi[0] = rtr2(pa, pa + 8 * 56); wphi[1] = rtr2(pa + 16 * 56, pa + 24 * 56);
        }
        SCHED();
        if (c == ADC && isA) {
#pragma nounroll
            for (int it = 0; it < 256 && __builtin_amdgcn_s_memtime() - clk0 < ADELAY; ++it) nap();
        }
        if (c > 0) { mk_(c - 1, 2); mv_(c - 1, 2); SCHED(); }
        issue(c, 0);
        if (c > 0) { mk_(c - 1, 3); mv_(c - 1, 3); SCHED(); }
        issue(c, 1);
        rd(c, 2); rd(c, 3);
        SCHED();
        mm(c, 0); SCHED(); issue(c, 2);
        mm(c, 1); SCHED(); issue(c, 3);
    };
    iter(ic<0>{}); iter(ic<1>{}); iter(ic<2>{}); iter(ic<3>{}); iter(ic<4>{}); iter(ic<5>{}); iter(ic<6>{});
    mm(6, 2); mm(6, 3);
    asm volatile("" : "+v"(gatev), "+v"(e0v));
    const u64 tg = (((u64)(u32)__builtin_amdgcn_readfirstlane(e0v.y) << 32) | (u32)__builtin_amdgcn_readfirstlane(e0v.x)) + 0x9E3779B97F4A7C15ull;
    const u32x2 tag = {(u32)tg, (u32)(tg >> 32)};
    STAMP(2);

    const u32 i2 = bi;
    const bool p2 = i2 < 28;
    const u32 u2 = xcd * 4 + i2 / 7, cb = i2 % 7;
    const u32 vwo = (w * 8 + (l >> 3)) * 896 + cb * 128 + (((l & 7) ^ (g << 1)) << 4);
    const u32 flo = l < 16 ? WS_FL + ((u2 >> 1) * 16 + l) * 8 : OOB;
    bool flags_ok = false;
    auto wo_dma = [&](const void* gp, u32 dst) {
        if (p2) {
            const rsrc_t ro = mk(gp, 448 * 896);
#pragma unroll
            for (u32 q = 0; q < 7; ++q) dma(lb + dst + q * 8192 + w * 1024, vwo, ro, q * (64 * 896));
        }
    };
    auto stage2 = [&]() {
        f32x16 aq = {};
        const u32 wq = opq(wa + L_WQ);
#pragma unroll
        for (int ks = 0; ks < 28; ++ks) aq = mm32(rtr2(wq + ks * 2048, wq + ks * 2048 + 512), xf[ks], aq);
        return aq;
    };
    const u32 ooff = ((t * 8 + rg * 2 + (tok >> 4)) * 16 + head) * 1024 + h * 256 + p * 16;
    auto publish_o = [&](f32x16 o, float r, u32 base) {
        o *= r;
        u32x4 o0 = pk8(o, 0), o1 = pk8(o, 1);
        if (h) { o1.z = 0; o1.w = 0; }
        gst(rs, base + ooff, o0); gst(rs, base + ooff + 512, o1);
    };
    const u32x4 kf0 = pk8(ak, 0), kf1 = pk8(ak, 1);
    u32x4 vf0 = pk8(av, 0), vf1 = pk8(av, 1);

    if (!isA) {
        const float keep = h ? 0.f : 1.f;
        {
            f32x16 pp = mm32(wphi[0], kf0, f32x16{});
            pp = mm32(wphi[1], kf1, pp);
            if (tok == 28) { vf0 = u32x4{0x3F803F80u, 0x3F803F80u, 0x3F803F80u, 0x3F803F80u}; vf1 = vf0; }
            w128(lb + L_VP + w * 2048 + l * 16, vf0); w128(lb + L_VP + w * 2048 + 1024 + l * 16, vf1);
            u32x4 ph[4];
            phi(pp, keep, ph);
            const u32 F = (((tok >> 1) & 3) << 2) | (((tok >> 3) & 1) << 1) | (tok & 1);
            const u32 pw = lb + L_PHI + w * 4096 + tok * 128 + ((h ^ F) << 3);
#pragma unroll
            for (int s = 0; s < 4; ++s) {
                const u32 cu = 8 * (s >> 1) + 2 * (s & 1);
                w64(pw ^ (cu << 3), u32x2{ph[s].x, ph[s].y});
                w64(pw ^ ((cu + 4) << 3), u32x2{ph[s].z, ph[s].w});
            }
        }
        LBAR();
        STAMP(3);
        {
            const u32 sign = rg >> 1, dt = rg & 1;
            const u32 F = ((2 * (g & 1) + (p >> 3)) << 2) | ((p >> 2) & 1);
            const u32 pr = lb + L_PHI + hh * 16384 + (16 * (g >> 1) + 4 * (g & 1) + (p >> 2)) * 128 + (((8 * sign + (p & 3)) ^ F) << 3);
            const u32 vr = lb + L_VP + hh * 8192 + g * 512 + (16 * dt + p) * 16;
            f32x4 c0 = {}, c1 = {};
#pragma unroll
            for (int r = 0; r < 4; ++r) {
                const u32x4 vv = r128(vr + r * 2048);
                c0 = mm16(rtr2(pr + r * 4096, (pr ^ 16) + r * 4096 + 1024), vv, c0);
                c1 = mm16(rtr2((pr ^ 32) + r * 4096, (pr ^ 48) + r * 4096 + 1024), vv, c1);
            }
            const u32 t0 = tag.x & 0xFFF, t1 = (tag.x >> 12) & 0xFFF, t2 = tag.y & 0xFFF, t3 = (tag.y >> 12) & 0xFFF;
            const u32 po = WS_P + (j * 8 + w) * 32768 + t * 1024 + l * 16;
#define TG(x, tt) (((u_(x) + 0x800u) & 0xFFFFF000u) | (tt))
            gst(rs, po, u32x4{TG(c0[0], t0), TG(c0[1], t1), TG(c0[2], t2), TG(c0[3], t3)});
            gst(rs, po + 16384, u32x4{TG(c1[0], t0), TG(c1[1], t1), TG(c1[2], t2), TG(c1[3], t3)});
        }
        STAMP(4);
        WAITBAR(2);
        STAMP(14);
        const u32x4 ones = {~0u, ~0u, ~0u, ~0u};
        gst(rs, WS_OB + ooff, ones); gst(rs, WS_OB + ooff + 512, ones);
        wo_dma(gwoa, L_WOA);
        const f32x16 aq = stage2();
        SCHED();
        STAMP(5);
        {
            const u32 un = t >> 1, cg = (t & 1) * 8 + w;
            const u32 po = WS_P + (j * 8 + un) * 32768 + (l >> 2) * 1024 + (4 * cg + (l & 3)) * 16;
            u32x4 a, bq;
        #pragma nounroll
    for (int it = 0; it < SPIN; ++it) {
                a = gld(rs, po); bq = gld(rs, po + 16384);
                const u32 bad = ((a.x ^ tag.x) | (bq.x ^ tag.x)) | ((a.y ^ (tag.x >> 12)) | (bq.y ^ (tag.x >> 12))) |
                                ((a.z ^ tag.y) | (bq.z ^ tag.y)) | ((a.w ^ (tag.y >> 12)) | (bq.w ^ (tag.y >> 12)));
                if (none((bad & 0xFFF) != 0)) break;
                nap();
            }
            STAMP(6);
            const u32 gq = cg >> 2;
            const u32 mb = gq == 3 ? 0u : 0xFFFFF000u;
            float uu[4];
#pragma unroll
            for (int i = 0; i < 4; ++i) {
                auto r = __builtin_amdgcn_permlane32_swap(a[i] & 0xFFFFF000u, bq[i] & mb, false, false);
                uu[i] = f_(r[0]) + f_(r[1]);
            }
            float ww[2];
#pragma unroll
            for (int i = 0; i < 2; ++i) {
                auto r = __builtin_amdgcn_permlane16_swap(u_(uu[i]), u_(uu[i + 2]), false, false);
                float y = f_(r[0]) + f_(r[1]);
                y += f_(__builtin_amdgcn_update_dpp(0, u_(y), 0x128, 0xf, 0xf, false));
                y += f_(__builtin_amdgcn_update_dpp(0, u_(y), 0x124, 0xf, 0xf, false));
                ww[i] = y;
            }
            const u32 pp = pk(ww[0], ww[1]), t16 = tag.x & 0xFFFF;
            const u32 s = ((un >> 1) & 1) * 2 + (gq >> 1);
            const u32 l32 = 16 * (un & 1) + ((4 * cg + (l & 3)) & 15) + 32 * (gq & 1);
            const u32 ko = WS_KVR + (((2 * j + (un >> 2)) * 4 + s) * 2 + (g >> 1)) * 1024 + l32 * 16 + (g & 1) * 8;
            gst64(rs, (l & 12) == 0 ? ko : OOB, u32x2{(pp << 16) | t16, (pp & 0xFFFF0000u) | t16});
        }
        WAITBAR(1);
        gst64(rs, tid == 0 ? WS_FL + (t * 16 + 8 + j) * 8 : OOB, tag);
        wo_dma(gwob, L_WOB);
        u32x4 pq[4];
        {
            f32x16 pp = mm32(wphi[0], pk8(aq, 0), f32x16{});
            pp = mm32(wphi[1], pk8(aq, 1), pp);
            phi(pp, keep, pq);
            asm volatile("" : "+v"(pq[0]), "+v"(pq[1]), "+v"(pq[2]), "+v"(pq[3]));
        }
        STAMP(15);
        {
            const u32 ko = WS_KVR + (head * 4 + rg) * 2048 + l * 16;
            u32x4 a, bq;
#pragma nounroll
            for (int it = 0; it < SPIN; ++it) {
                a = gld(rs, ko); bq = gld(rs, ko + 1024);
                const u32x2 fv = __builtin_amdgcn_raw_buffer_load_b64(rs, flo, 0, SC1);
                const u32 bad = ((a.x ^ tag.x) | (bq.x ^ tag.x)) | ((a.y ^ tag.x) | (bq.y ^ tag.x)) | ((a.z ^ tag.x) | (bq.z ^ tag.x)) | ((a.w ^ tag.x) | (bq.w ^ tag.x));
                flags_ok = none(l < 16 && (fv.x != tag.x || fv.y != tag.y));
                if (none((bad & 0xFFFF) != 0)) break;
                nap();
            }
            STAMP(7);
#define HI2(x, y) __builtin_amdgcn_perm(y, x, 0x07060302u)
            w128(lb + L_KVL + w * 1024 + l * 16, u32x4{HI2(a.x, a.y), HI2(a.z, a.w), HI2(bq.x, bq.y), HI2(bq.z, bq.w)});
            LBAR();
            f32x16 o = {};
#pragma unroll
            for (int s = 0; s < 4; ++s) o = mm32(r128(lb + L_KVL + (hh * 4 + s) * 1024 + l * 16), pq[s], o);
            auto r = __builtin_amdgcn_permlane32_swap(u_(o[12]), u_(o[12]), false, false);
            publish_o(o, __builtin_amdgcn_rcpf(f_(r[1]) + 1e-6f), WS_OB);
        }
        STAMP(8);
    } else {
        const u32 kxo = WS_KX + (t * 16 + head) * 3072 + l * 16;
        if (rg < 3) {
            const u32 ax = lb + L_AX + w * 3072 + l * 16;
            w128(ax, kf0); w128(ax + 1024, kf1); w128(ax + 2048, vf1);
        } else {
            gst(rs, kxo, kf0); gst(rs, kxo + 1024, kf1); gst(rs, kxo + 2048, vf1);
            WAITVM(0);
            gst64(rs, l == 0 ? WS_KF + (t * 16 + head) * 8 : OOB, tag);
        }
        WAITBAR(0);
        STAMP(3);
        if (j == 0) {
            if (tid < 448) w128(lb + L_WQ + tid * 128 + ((7 ^ ((tid & 2) << 1)) << 4), u32x4{gatev, 0, 0, 0});
            LBAR();
        }
        wo_dma(gwoa, L_WOA);
        const f32x16 aq = stage2();
        STAMP(4);
        if (j == 0 && hh == 1) {
            const float gg = __builtin_amdgcn_rcpf(1.f + __builtin_amdgcn_exp2f(aq[12] * -LOG2E));
            __builtin_amdgcn_raw_buffer_store_b32(u_(gg), rs, h ? WS_G + (t * 128 + rg * 32 + tok) * 4 : OOB, 0, SC1);
        }
        u32x4 qf0 = pk8(aq, 0), qf1 = pk8(aq, 1);
        if (h) { qf1.z = 0; qf1.w = 0; }
        u32x4 kp0, kp1, vp;
        bool hasprev = true;
        if (rg > 0) {
            const u32 ax = lb + L_AX + (w - 1) * 3072 + l * 16;
            kp0 = r128(ax); kp1 = r128(ax + 1024); vp = r128(ax + 2048);
        } else if (t > 0) {
            wait_flags(rs, l == 0 ? WS_KF + ((t - 1) * 16 + head) * 8 : OOB, tag);
            kp0 = gld(rs, kxo - 16 * 3072); kp1 = gld(rs, kxo - 16 * 3072 + 1024); vp = gld(rs, kxo - 16 * 3072 + 2048);
        } else {
            kp0 = u32x4{}; kp1 = u32x4{}; vp = u32x4{}; hasprev = false;
        }
        STAMP(5);
        f32x16 so = mm32(kf0, qf0, f32x16{}); so = mm32(kf1, qf1, so);
        f32x16 sp = mm32(kp0, qf0, f32x16{}); sp = mm32(kp1, qf1, sp);
        const u32 om = opq(0u - (tok & 1));
        const u32 base = (tok & ~1u) - 4 * h, basep = hasprev ? base + 32 : 1000u;
        float sv[12];
        const float NEG = -1e30f;
#pragma unroll
        for (int i = 0; i < 8; ++i) { const float v = sel(om, so[2 * i + 1], so[2 * i]); sv[i] = (base - (8 * (i >> 1) + 2 * (i & 1))) <= 14u ? v : NEG; }
#pragma unroll
        for (int i = 4; i < 8; ++i) { const float v = sel(om, sp[2 * i + 1], sp[2 * i]); sv[4 + i] = (basep - (8 * (i >> 1) + 2 * (i & 1))) <= 14u ? v : NEG; }
        float mx = fmaxf(sv[0], sv[1]);
#pragma unroll
        for (int i = 2; i < 12; i += 2) mx = fmaxf(fmaxf(mx, sv[i]), sv[i + 1]);
        const float sc = 0.18898223650461362f * LOG2E;
        mx = xmax32(mx) * -sc;
        float sum = 0.f;
        u32 pd[12];
        const u32 pm = om ^ 0x0000FFFFu;
#pragma unroll
        for (int i = 0; i < 12; ++i) { const float e = __builtin_amdgcn_exp2f(fmaf(sv[i], sc, mx)); sum += e; pd[i] = pk(e, e) & pm; }
        f32x16 o = mm32(vf0, u32x4{pd[0], pd[1], pd[2], pd[3]}, f32x16{});
        o = mm32(vf1, u32x4{pd[4], pd[5], pd[6], pd[7]}, o);
        o = mm32(vp, u32x4{pd[8], pd[9], pd[10], pd[11]}, o);
        publish_o(o, __builtin_amdgcn_rcpf(xadd32(sum)), WS_OA);
        STAMP(6);
        WAITBAR(0);
        gst64(rs, tid == 0 ? WS_FL + (t * 16 + j) * 8 : OOB, tag);
        wo_dma(gwob, L_WOB);
        STAMP(8);
    }

    if (p2) {
        const u32 tt = w & 3, kh = w >> 2;
        if (!flags_ok) wait_flags(rs, flo, tag);
        if (isA) WAITBAR(0);
        STAMP(9);
        const u32 oo = ((u2 * 4 + tt) * 16 + kh * 8) * 1024 + l * 16;
        u32x4 of[8];
#pragma unroll
        for (int e = 0; e < 8; ++e) of[e] = gld(rs, WS_OA + oo + e * 1024);
        const float gg = f_(__builtin_amdgcn_raw_buffer_load_b32(rs, WS_G + (u2 * 64 + tt * 16 + p) * 4, 0, SC1));
        const u32 f0 = 2 * (g & 1) + (p >> 3);
        const u32 wb0 = lb + (224 * kh + 16 * (g >> 1) + 4 * (g & 1) + (p >> 2)) * 128 + ((((p & 3) >> 1) ^ (2 * f0)) << 4) + (p & 1) * 8;
        const u32 wb1 = wb0 + (g == 3 ? 0 : 1024);
        u32 wr0[4], wr1[4];
        auto wbase = [&](u32 img) {
#pragma unroll
            for (int x = 0; x < 4; ++x) { wr0[x] = opq((wb0 + img) ^ (x << 5)); wr1[x] = opq((wb1 + img) ^ (x << 5)); }
        };
        auto wfrag = [&](int e, int nt) {
            const int x = nt ^ (2 * (e & 1));
            return rtr2(wr0[x] + e * 3584, wr1[x] + e * 3584);
        };
        wbase(L_WOA);
        f32x4 ca[4] = {}, cbb[4] = {};
        u32x4 ob[8];
#pragma unroll
        for (int e = 0; e < 8; ++e) {
#pragma unroll
            for (int nt = 0; nt < 4; ++nt) ca[nt] = mm16(wfrag(e, nt), of[e], ca[nt]);
            if (e == 3) {
#pragma unroll
                for (int q = 0; q < 8; ++q) ob[q] = gld(rs, WS_OB + oo + q * 1024);
            }
        }
        STAMP(10);
        u32x4 wf[8][4];
        wbase(L_WOB);
#pragma unroll
        for (int e = 0; e < 8; ++e)
#pragma unroll
            for (int nt = 0; nt < 4; ++nt) wf[e][nt] = wfrag(e, nt);
#pragma nounroll
        for (int it = 0; it < SPIN; ++it) {
            u32 m = 0;
#pragma unroll
            for (int e = 0; e < 8; ++e) m = max(max(m, max(ob[e].x, ob[e].y)), max(ob[e].z, ob[e].w));
            if (none(m == ~0u)) break;
#pragma nounroll
            for (; it < SPIN; ++it) {
                const u32x4 v = gld(rs, WS_OB + oo + 7 * 1024);
                if (none(max(max(v.x, v.y), max(v.z, v.w)) == ~0u)) break;
                nap();
            }
#pragma unroll
            for (int e = 0; e < 8; ++e) ob[e] = gld(rs, WS_OB + oo + e * 1024);
        }
        STAMP(11);
#pragma unroll
        for (int e = 0; e < 8; ++e)
#pragma unroll
            for (int nt = 0; nt < 4; ++nt) cbb[nt] = mm16(wf[e][nt], ob[e], cbb[nt]);
        const u32 xo = lb + L_XO + kh * 16384 + (tt * 16 + p) * 256 + ((g ^ (p & 7)) << 4);
#pragma unroll
        for (int nt = 0; nt < 4; ++nt) {
            const f32x4 r = ca[nt] * gg + cbb[nt] * (1.f - gg);
            w128(xo ^ (nt << 6), __builtin_bit_cast(u32x4, r));
        }
        LBAR();
        const u32 tk = tid >> 3, c8 = tid & 7;
        const u32 xr = lb + L_XO + tk * 256 + (((2 * c8) ^ (tk & 7)) << 4);
        const f32x4 r0 = __builtin_bit_cast(f32x4, r128(xr)) + __builtin_bit_cast(f32x4, r128(xr + 16384));
        const f32x4 r1 = __builtin_bit_cast(f32x4, r128(xr ^ 16)) + __builtin_bit_cast(f32x4, r128((xr ^ 16) + 16384));
        __builtin_amdgcn_raw_buffer_store_b128(u32x4{pk(r0[0], r0[1]), pk(r0[2], r0[3]), pk(r1[0], r1[1]), pk(r1[2], r1[3])},
                                               mk(gout, 2048 * 896), (u2 * 64 + tk) * 896 + cb * 128 + c8 * 16, 0, OUTAUX);
        STAMP(12);
    } else if (b == 255 && w < 4) {
        wait_flags(rs, WS_FL + tid * 8, tag);
        asm volatile("s_barrier" ::: "memory");
        gst64(rs, tid == 0 ? WS_E0 : OOB, tag);
    } else if (b == 255) {
        asm volatile("s_barrier" ::: "memory");
    }
    asm volatile(".Lfm_end:" ::: "memory");
}

extern "C" void kernel_launch(void* const* d_in, const int* in_sizes, int n_in, void* d_out, int out_size, void* d_ws, size_t ws_size, hipStream_t stream) {
    (void)in_sizes; (void)n_in; (void)out_size; (void)ws_size;
    Args a;
    a.x = d_in[0]; a.wa = d_in[1]; a.wb = d_in[4]; a.wphi = d_in[5]; a.gate = d_in[7]; a.ws = d_ws; a.woa = d_in[2]; a.wob = d_in[6]; a.out = d_out; a.pad = nullptr;
    hipLaunchKernelGGL(fused, dim3(256), dim3(512), 0, stream, a);
}
```

```cpp
#include <hip/hip_runtime.h>
#include <stdint.h>

typedef unsigned u32;
typedef unsigned long long u64;
typedef u32 u32x4 __attribute__((ext_vector_type(4)));
typedef u32 u32x2 __attribute__((ext_vector_type(2)));
typedef float f32x2 __attribute__((ext_vector_type(2)));
typedef float f32x4 __attribute__((ext_vector_type(4)));
typedef float f32x16 __attribute__((ext_vector_type(16)));
typedef __bf16 bf16x8 __attribute__((ext_vector_type(8)));
typedef __bf16 bf16x2 __attribute__((ext_vector_type(2)));
typedef short s16x4 __attribute__((ext_vector_type(4)));
typedef __amdgpu_buffer_rsrc_t rsrc_t;
#define LDSP(T) __attribute__((address_space(3))) T*
#define DI __device__ __forceinline__

constexpr u32 WS_E0 = 0;
constexpr u32 WS_FL = 256;
constexpr u32 WS_KF = 4096;
constexpr u32 WS_G = 8192;
constexpr u32 WS_KVR = 16384;
constexpr u32 WS_KX = 256 * 1024;
constexpr u32 WS_P = 1024 * 1024;
constexpr u32 WS_OA = 3 * 1024 * 1024;
constexpr u32 WS_OB = 5 * 1024 * 1024;
constexpr u32 WS_BYTES = 7 * 1024 * 1024;
constexpr u32 L_WQ = 104 * 1024, L_WOA = 48 * 1024, L_WOB = 104 * 1024;
constexpr u32 L_PHI = 0, L_VP = 32 * 1024, L_WPHI = 128 * 1024, L_JUNK = 144 * 1024;
constexpr u32 L_AX = 0, L_KVL = 0, L_XO = 8 * 1024;
constexpr u32 OOB = 0x80000000u;
constexpr int SPIN = 1 << 15;
constexpr int SC1 = 16;
#define ADELAY 3500
#define ADC 0
#define PRO 2
#define WARM 1
template <int N> struct ic { static constexpr int value = N; };
constexpr float LOG2E = 1.4426950408889634f;

DI void dma(u32 m0v, u32 voff, rsrc_t rs, u32 soff) {
    asm volatile("s_mov_b32 m0, %0\n\ts_nop 0\n\tbuffer_load_dwordx4 %1, %2, %3 offen lds" ::"s"(m0v), "v"(voff), "s"(rs), "s"(soff) : "memory");
}
DI void dma4(u32 m0v, u32 voff, rsrc_t rs) {
    asm volatile("s_mov_b32 m0, %0\n\ts_nop 0\n\tbuffer_load_dword %1, %2, 0 offen lds" ::"s"(m0v), "v"(voff), "s"(rs) : "memory");
}
template <int N> DI void waitbar() { asm volatile("s_waitcnt vmcnt(%0) lgkmcnt(0)\n\ts_barrier" ::"i"(N) : "memory"); }
#define WAITBAR(N) asm volatile("s_waitcnt vmcnt(" #N ") lgkmcnt(0)\n\ts_barrier" ::: "memory")
#define LBAR() asm volatile("s_waitcnt lgkmcnt(0)\n\ts_barrier" ::: "memory")
#define WAITVM(N) asm volatile("s_waitcnt vmcnt(" #N ")" ::: "memory")
#define SCHED() __builtin_amdgcn_sched_barrier(0)
DI u32x4 r128(u32 a) { return *(LDSP(const u32x4))(size_t)a; }
DI void w128(u32 a, u32x4 v) { *(LDSP(u32x4))(size_t)a = v; }
DI void w64(u32 a, u32x2 v) { *(LDSP(u32x2))(size_t)a = v; }
DI u32x2 rtr(u32 a) { return __builtin_bit_cast(u32x2, __builtin_amdgcn_ds_read_tr16_b64_v4i16((LDSP(s16x4))(size_t)a)); }
DI u32x4 rtr2(u32 a, u32 b) { u32x2 x = rtr(a), y = rtr(b); return u32x4{x.x, x.y, y.x, y.y}; }
DI f32x16 mm32(u32x4 a, u32x4 b, f32x16 c) { return __builtin_amdgcn_mfma_f32_32x32x16_bf16(__builtin_bit_cast(bf16x8, a), __builtin_bit_cast(bf16x8, b), c, 0, 0, 0); }
DI f32x4 mm16(u32x4 a, u32x4 b, f32x4 c) { return __builtin_amdgcn_mfma_f32_16x16x32_bf16(__builtin_bit_cast(bf16x8, a), __builtin_bit_cast(bf16x8, b), c, 0, 0, 0); }
DI u32 pk(float lo, float hi) { f32x2 v = {lo, hi}; return __builtin_bit_cast(u32, __builtin_convertvector(v, bf16x2)); }
DI u32x4 pk8(const f32x16& a, int s) { return u32x4{pk(a[8 * s], a[8 * s + 1]), pk(a[8 * s + 2], a[8 * s + 3]), pk(a[8 * s + 4], a[8 * s + 5]), pk(a[8 * s + 6], a[8 * s + 7])}; }
DI float f_(u32 x) { return __builtin_bit_cast(float, x); }
DI u32 u_(float x) { return __builtin_bit_cast(u32, x); }
DI float xmax32(float x) { auto r = __builtin_amdgcn_permlane32_swap(u_(x), u_(x), false, false); return fmaxf(f_(r[0]), f_(r[1])); }
DI float xmin32(float x) { auto r = __builtin_amdgcn_permlane32_swap(u_(x), u_(x), false, false); return fminf(f_(r[0]), f_(r[1])); }
DI float xadd32(float x) { auto r = __builtin_amdgcn_permlane32_swap(u_(x), u_(x), false, false); return f_(r[0]) + f_(r[1]); }
DI u32x4 gld(rsrc_t rs, u32 off) { return __builtin_amdgcn_raw_buffer_load_b128(rs, off, 0, SC1); }
DI void gst(rsrc_t rs, u32 off, u32x4 v) { __builtin_amdgcn_raw_buffer_store_b128(v, rs, off, 0, SC1); }
DI void gst64(rsrc_t rs, u32 off, u32x2 v) { __builtin_amdgcn_raw_buffer_store_b64(v, rs, off, 0, SC1); }
DI u32 opq(u32 x) { asm("" : "+v"(x)); return x; }
DI float sel(u32 m, float a, float b) { return f_((u_(a) & m) | (u_(b) & ~m)); }
DI void nap() { asm volatile("s_sleep 1" ::: "memory"); }
DI bool none(bool bad) { return __builtin_amdgcn_ballot_w64(bad) == 0; }
DI rsrc_t mk(const void* p, u32 bytes) { return __builtin_amdgcn_make_buffer_rsrc((void*)p, 0, bytes, 0x00020000); }
DI void wait_flags(rsrc_t rs, u32 off, u32x2 tag) {
#pragma nounroll
    for (int it = 0; it < SPIN; ++it) {
        u32x2 v = __builtin_amdgcn_raw_buffer_load_b64(rs, off, 0, SC1);
        if (none(off != OOB && (v.x != tag.x || v.y != tag.y))) break;
        nap();
    }
    asm volatile("" ::: "memory");
}

DI void phi(const f32x16& p, float keep, u32x4 (&o)[4]) {
    float mx = fmaxf(p[0], p[1]), mn = fminf(p[0], p[1]);
#pragma unroll
    for (int i = 2; i < 16; i += 2) { mx = fmaxf(fmaxf(mx, p[i]), p[i + 1]); mn = fminf(fminf(mn, p[i]), p[i + 1]); }
    mx = xmax32(mx) * -LOG2E; mn = xmin32(mn) * LOG2E;
    f32x16 a, b;
#pragma unroll
    for (int i = 0; i < 16; ++i) { a[i] = __builtin_amdgcn_exp2f(fmaf(p[i], LOG2E, mx)); b[i] = __builtin_amdgcn_exp2f(fmaf(p[i], -LOG2E, mn)); }
    float sa = ((a[12] + a[13]) + (a[14] + a[15])) * keep, sb = ((b[12] + b[13]) + (b[14] + b[15])) * keep;
#pragma unroll
    for (int i = 0; i < 12; i += 4) { sa += (a[i] + a[i + 1]) + (a[i + 2] + a[i + 3]); sb += (b[i] + b[i + 1]) + (b[i + 2] + b[i + 3]); }
    sa = __builtin_amdgcn_rcpf(xadd32(sa)); sb = __builtin_amdgcn_rcpf(xadd32(sb));
    a *= sa; b *= sb;
    o[0] = pk8(a, 0); o[1] = pk8(a, 1); o[2] = pk8(b, 0); o[3] = pk8(b, 1);
}

#define STAMP(i)

struct Args { const void *x, *wa, *wb, *wphi, *gate; void* ws; const void *woa, *wob; void* out; const void* pad; };
__global__ __launch_bounds__(512, 2) void fused(const Args ar) {
    __shared__ __attribute__((aligned(1024))) char smem[163840];
    STAMP(0);
    const u32 tid = threadIdx.x, l = tid & 63;
    const u32 w = __builtin_amdgcn_readfirstlane(tid >> 6);
    const u32 lb = (u32)(size_t)(LDSP(char))smem;
    {
        u64 pc; u32 len;
        asm volatile("s_getpc_b64 %0\n.Lfm_begin:\n\ts_mov_b32 %1, .Lfm_end-.Lfm_begin\n\ts_max_i32 %1, %1, 0\n\ts_nop 4" : "=s"(pc), "=s"(len)::"scc");
        dma(lb + L_JUNK + w * 1024, tid * 128, mk((const void*)pc, len), 0);
    }
    const void *gx = ar.x, *gwa = ar.wa, *gwb = ar.wb, *gwphi = ar.wphi, *ggate = ar.gate, *gwoa = ar.woa, *gwob = ar.wob;
    void *gws = ar.ws, *gout = ar.out;
    asm volatile("" : "+s"(gx), "+s"(gwa), "+s"(gwb), "+s"(gwphi), "+s"(ggate), "+s"(gws), "+s"(gwoa), "+s"(gwob), "+s"(gout));
    const u64 clk0 = __builtin_amdgcn_s_memtime();
    const u32 b = blockIdx.x, xcd = b & 7, bi = b >> 3;
    const bool isA = bi >= 16;
    const u32 t = (xcd >> 1) * 4 + ((bi & 15) >> 2), j = (xcd & 1) * 4 + (bi & 3);
    const u32 rg = w & 3, hh = w >> 2, head = 2 * j + hh;
    const u32 tok = l & 31, h = l >> 5, p = l & 15, g = l >> 4;
    const rsrc_t rx = mk(gx, 2048 * 896), rw = mk(isA ? gwa : gwb, 448 * 2688), rs = mk(gws, WS_BYTES);

    const u32 vx0 = (t * 128 + w * 16 + (l >> 3)) * 896 + (((l & 7) ^ g) << 4);
    const u32 vx1 = (vx0 + 8 * 896) ^ 64;
    const u32 wsrc = (l & 7) ^ ((g & 1) << 2);
    const u32 vw = wsrc == 7 ? OOB : (w * 8 + (l >> 3)) * 2688 + j * 112 + wsrc * 16;
    auto chunk1 = [&](u32 c, int q) {
        const u32 sl = lb + (c & 3) * 32768;
        if (q == 0) dma(sl + w * 2048, vx0, rx, c * 128);
        if (q == 1) dma(sl + w * 2048 + 1024, vx1, rx, c * 128);
        if (q == 2) dma(sl + 16384 + w * 1024, vw, rw, c * (64 * 2688) + 896);
        if (q == 3) dma(sl + 24576 + w * 1024, vw, rw, c * (64 * 2688) + 1792);
    };
    auto chunk = [&](u32 c) { chunk1(c, 0); chunk1(c, 1); chunk1(c, 2); chunk1(c, 3); };
    if (isA) {
        const u32 n = ((bi & 15) * 8 + w) * 64 + l, jb = (xcd & 1) * 3;
        const u32 n1 = n - 3584, n2 = n - 5376;
        dma4(lb + L_JUNK + 8192 + w * 256, n < 3584 ? (t & 12) * (128 * 896) + n * 128 : OOB, rx);
        const rsrc_t rwb = mk(gwb, 448 * 2688);
        dma4(lb + L_JUNK + 8192 + w * 256, n1 < 3584 ? (n1 >> 3) * 2688 + (((n1 >> 2) & 1) * 7 + 7 + jb + (n1 & 3)) * 128 : OOB, rwb);
        dma4(lb + L_JUNK + 8192 + w * 256, n < 5376 ? (n >> 2) * 896 + (jb + (n & 3)) * 128 : OOB, rw);
        dma4(lb + L_JUNK + 8192 + w * 256, n2 < 1792 ? (n2 >> 2) * 2688 + (jb + (n2 & 3)) * 128 : OOB, rwb);
    }
    chunk(0);
    u32 gatev; u32x2 e0v;
    asm volatile("buffer_load_dwordx2 %0, off, %1, 0 sc1" : "=v"(e0v) : "s"(rs) : "memory");
    asm volatile("buffer_load_ushort %0, %1, %2, 0 offen" : "=v"(gatev) : "v"(tid * 2), "s"(mk(ggate, 896)) : "memory");
    {
        const rsrc_t rp = mk(gwphi, isA ? 0 : 16 * 1568);
        dma(lb + L_WPHI + w * 2048, l * 16, rp, head * 1568);
        dma(lb + L_WPHI + w * 2048 + 1024, l < 34 ? l * 16 + 1024 : OOB, rp, head * 1568);
    }
    chunk(1);
    if (PRO == 3) chunk(2);
    STAMP(13);

    const u32 xa = lb + (rg * 32 + tok) * 128 + ((h ^ ((tok >> 1) & 7)) << 4);
    const u32 wa = lb + (8 * h + (p >> 2)) * 128 + ((56 * hh + 32 * (g & 1) + 8 * (p & 3)) ^ (((p >> 3) & 1) << 6));

    const u32 xah = opq(xa + 65536), wah = opq(wa + 65536);
    u32x4 xf[28], wphi[2];
    f32x16 ak = {}, av = {};
    u32x4 wk[4], wv[4];
    auto rd = [&](int c, int s) {
        const u32 sl = (c & 1) * 32768, xb = (c & 2) ? xah : xa, wb = (c & 2) ? wah : wa;
        xf[4 * c + s] = r128((xb ^ (32 * s)) + sl);
        wk[s] = rtr2(wb + sl + 16384 + s * 2048, wb + sl + 16384 + s * 2048 + 512);
        wv[s] = rtr2(wb + sl + 24576 + s * 2048, wb + sl + 24576 + s * 2048 + 512);
    };
    auto mm = [&](int c, int s) { ak = mm32(wk[s], xf[4 * c + s], ak); av = mm32(xf[4 * c + s], wv[s], av); };
    auto wq_dma = [&](u32 q) { dma(lb + L_WQ + q * 8192 + w * 1024, vw, rw, q * (64 * 2688)); };
    auto mk_ = [&](int c, int s) { ak = mm32(wk[s], xf[4 * c + s], ak); };
    auto mv_ = [&](int c, int s) { av = mm32(xf[4 * c + s], wv[s], av); };
    auto issue = [&](int c, int q) {
        if (c + PRO < 7) chunk1(c + PRO, q);
        else if (c + PRO == 7) wq_dma(3 + q);
        else if (c + PRO == 8 && q < 3) wq_dma(q);
        SCHED();
    };
    auto iter = [&](auto cc) {
        constexpr int c = decltype(cc)::value;
        constexpr int issued = 4 * (PRO + c) + 4 - (PRO + c > 8 ? 1 : 0) - (PRO + c > 9 ? 4 * (PRO + c - 9) : 0);
        constexpr int upto = 4 * (c + 1) + (c > 0 ? 4 : 0);
        waitbar<issued - upto>();
        STAMP(16 + c);
        rd(c, 0); rd(c, 1);
        if (c == 1) {
            const u32 fq = 4 * (g & 1) + (p & 3);
            const u32 pa = lb + L_WPHI + w * 2048 + (4 * h + (p >> 2)) * 56 + (fq == 7 ? 6 : fq) * 8;
            wphi[0] = rtr2(pa, pa + 8 * 56); wphi[1] = rtr2(pa + 16 * 56, pa + 24 * 56);
        }
        SCHED();
        if (c == ADC && isA) {
#pragma nounroll
            for (int it = 0; it < 256 && __builtin_amdgcn_s_memtime() - clk0 < ADELAY; ++it) nap();
        }
        if (c > 0) { mk_(c - 1, 2); mv_(c - 1, 2); SCHED(); }
        issue(c, 0);
        if (c > 0) { mk_(c - 1, 3); mv_(c - 1, 3); SCHED(); }
        issue(c, 1);
        rd(c, 2); rd(c, 3);
        SCHED();
        mm(c, 0); SCHED(); issue(c, 2);
        mm(c, 1); SCHED(); issue(c, 3);
    };
    iter(ic<0>{}); iter(ic<1>{}); iter(ic<2>{}); iter(ic<3>{}); iter(ic<4>{}); iter(ic<5>{}); iter(ic<6>{});
    mm(6, 2); mm(6, 3);
    asm volatile("" : "+v"(gatev), "+v"(e0v));
    const u64 tg = (((u64)(u32)__builtin_amdgcn_readfirstlane(e0v.y) << 32) | (u32)__builtin_amdgcn_readfirstlane(e0v.x)) + 0x9E3779B97F4A7C15ull;
    const u32x2 tag = {(u32)tg, (u32)(tg >> 32)};
    STAMP(2);

    const u32 i2 = bi;
    const bool p2 = i2 < 28;
    const u32 u2 = xcd * 4 + i2 / 7, cb = i2 % 7;
    const u32 vwo = (w * 8 + (l >> 3)) * 896 + cb * 128 + (((l & 7) ^ (g << 1)) << 4);
    const u32 flo = l < 16 ? WS_FL + ((u2 >> 1) * 16 + l) * 8 : OOB;
    bool flags_ok = false;
    auto wo_dma = [&](const void* gp, u32 dst) {
        if (p2) {
            const rsrc_t ro = mk(gp, 448 * 896);
#pragma unroll
            for (u32 q = 0; q < 7; ++q) dma(lb + dst + q * 8192 + w * 1024, vwo, ro, q * (64 * 896));
        }
    };
    auto stage2 = [&]() {
        f32x16 aq = {};
        const u32 wq = opq(wa + L_WQ);
#pragma unroll
        for (int ks = 0; ks < 28; ++ks) aq = mm32(rtr2(wq + ks * 2048, wq + ks * 2048 + 512), xf[ks], aq);
        return aq;
    };
    const u32 ooff = ((t * 8 + rg * 2 + (tok >> 4)) * 16 + head) * 1024 + h * 256 + p * 16;
    auto publish_o = [&](f32x16 o, float r, u32 base) {
        o *= r;
        u32x4 o0 = pk8(o, 0), o1 = pk8(o, 1);
        if (h) { o1.z = 0; o1.w = 0; }
        gst(rs, base + ooff, o0); gst(rs, base + ooff + 512, o1);
    };
    const u32x4 kf0 = pk8(ak, 0), kf1 = pk8(ak, 1);
    u32x4 vf0 = pk8(av, 0), vf1 = pk8(av, 1);

    if (!isA) {
        const float keep = h ? 0.f : 1.f;
        {
            f32x16 pp = mm32(wphi[0], kf0, f32x16{});
            pp = mm32(wphi[1], kf1, pp);
            if (tok == 28) { vf0 = u32x4{0x3F803F80u, 0x3F803F80u, 0x3F803F80u, 0x3F803F80u}; vf1 = vf0; }
            w128(lb + L_VP + w * 2048 + l * 16, vf0); w128(lb + L_VP + w * 2048 + 1024 + l * 16, vf1);
            u32x4 ph[4];
            phi(pp, keep, ph);
            const u32 F = (((tok >> 1) & 3) << 2) | (((tok >> 3) & 1) << 1) | (tok & 1);
            const u32 pw = lb + L_PHI + w * 4096 + tok * 128 + ((h ^ F) << 3);
#pragma unroll
            for (int s = 0; s < 4; ++s) {
                const u32 cu = 8 * (s >> 1) + 2 * (s & 1);
                w64(pw ^ (cu << 3), u32x2{ph[s].x, ph[s].y});
                w64(pw ^ ((cu + 4) << 3), u32x2{ph[s].z, ph[s].w});
            }
        }
        LBAR();
        STAMP(3);
        {
            const u32 sign = rg >> 1, dt = rg & 1;
            const u32 F = ((2 * (g & 1) + (p >> 3)) << 2) | ((p >> 2) & 1);
            const u32 pr = lb + L_PHI + hh * 16384 + (16 * (g >> 1) + 4 * (g & 1) + (p >> 2)) * 128 + (((8 * sign + (p & 3)) ^ F) << 3);
            const u32 vr = lb + L_VP + hh * 8192 + g * 512 + (16 * dt + p) * 16;
            f32x4 c0 = {}, c1 = {};
#pragma unroll
            for (int r = 0; r < 4; ++r) {
                const u32x4 vv = r128(vr + r * 2048);
                c0 = mm16(rtr2(pr + r * 4096, (pr ^ 16) + r * 4096 + 1024), vv, c0);
                c1 = mm16(rtr2((pr ^ 32) + r * 4096, (pr ^ 48) + r * 4096 + 1024), vv, c1);
            }
            const u32 t0 = tag.x & 0xFFF, t1 = (tag.x >> 12) & 0xFFF, t2 = tag.y & 0xFFF, t3 = (tag.y >> 12) & 0xFFF;
            const u32 po = WS_P + (j * 8 + w) * 32768 + t * 1024 + l * 16;
#define TG(x, tt) (((u_(x) + 0x800u) & 0xFFFFF000u) | (tt))
            gst(rs, po, u32x4{TG(c0[0], t0), TG(c0[1], t1), TG(c0[2], t2), TG(c0[3], t3)});
            gst(rs, po + 16384, u32x4{TG(c1[0], t0), TG(c1[1], t1), TG(c1[2], t2), TG(c1[3], t3)});
        }
        STAMP(4);
        WAITBAR(2);
        STAMP(14);
        const u32x4 ones = {~0u, ~0u, ~0u, ~0u};
        gst(rs, WS_OB + ooff, ones); gst(rs, WS_OB + ooff + 512, ones);
        wo_dma(gwoa, L_WOA);
        const f32x16 aq = stage2();
        SCHED();
        STAMP(5);
        {
            const u32 un = t >> 1, cg = (t & 1) * 8 + w;
            const u32 po = WS_P + (j * 8 + un) * 32768 + (l >> 2) * 1024 + (4 * cg + (l & 3)) * 16;
            u32x4 a, bq;
        #pragma nounroll
    for (int it = 0; it < SPIN; ++it) {
                a = gld(rs, po); bq = gld(rs, po + 16384);
                const u32 bad = ((a.x ^ tag.x) | (bq.x ^ tag.x)) | ((a.y ^ (tag.x >> 12)) | (bq.y ^ (tag.x >> 12))) |
                                ((a.z ^ tag.y) | (bq.z ^ tag.y)) | ((a.w ^ (tag.y >> 12)) | (bq.w ^ (tag.y >> 12)));
                if (none((bad & 0xFFF) != 0)) break;
                nap();
            }
            STAMP(6);
            const u32 gq = cg >> 2;
            const u32 mb = gq == 3 ? 0u : 0xFFFFF000u;
            float uu[4];
#pragma unroll
            for (int i = 0; i < 4; ++i) {
                auto r = __builtin_amdgcn_permlane32_swap(a[i] & 0xFFFFF000u, bq[i] & mb, false, false);
                uu[i] = f_(r[0]) + f_(r[1]);
            }
            float ww[2];
#pragma unroll
            for (int i = 0; i < 2; ++i) {
                auto r = __builtin_amdgcn_permlane16_swap(u_(uu[i]), u_(uu[i + 2]), false, false);
                float y = f_(r[0]) + f_(r[1]);
                y += f_(__builtin_amdgcn_update_dpp(0, u_(y), 0x128, 0xf, 0xf, false));
                y += f_(__builtin_amdgcn_update_dpp(0, u_(y), 0x124, 0xf, 0xf, false));
                ww[i] = y;
            }
            const u32 pp = pk(ww[0], ww[1]), t16 = tag.x & 0xFFFF;
            const u32 s = ((un >> 1) & 1) * 2 + (gq >> 1);
            const u32 l32 = 16 * (un & 1) + ((4 * cg + (l & 3)) & 15) + 32 * (gq & 1);
            const u32 ko = WS_KVR + (((2 * j + (un >> 2)) * 4 + s) * 2 + (g >> 1)) * 1024 + l32 * 16 + (g & 1) * 8;
            gst64(rs, (l & 12) == 0 ? ko : OOB, u32x2{(pp << 16) | t16, (pp & 0xFFFF0000u) | t16});
        }
        WAITBAR(1);
        gst64(rs, tid == 0 ? WS_FL + (t * 16 + 8 + j) * 8 : OOB, tag);
        wo_dma(gwob, L_WOB);
        u32x4 pq[4];
        {
            f32x16 pp = mm32(wphi[0], pk8(aq, 0), f32x16{});
            pp = mm32(wphi[1], pk8(aq, 1), pp);
            phi(pp, keep, pq);
            asm volatile("" : "+v"(pq[0]), "+v"(pq[1]), "+v"(pq[2]), "+v"(pq[3]));
        }
        STAMP(15);
        {
            const u32 ko = WS_KVR + (head * 4 + rg) * 2048 + l * 16;
            u32x4 a, bq;
#pragma nounroll
            for (int it = 0; it < SPIN; ++it) {
                a = gld(rs, ko); bq = gld(rs, ko + 1024);
                const u32x2 fv = __builtin_amdgcn_raw_buffer_load_b64(rs, flo, 0, SC1);
                const u32 bad = ((a.x ^ tag.x) | (bq.x ^ tag.x)) | ((a.y ^ tag.x) | (bq.y ^ tag.x)) | ((a.z ^ tag.x) | (bq.z ^ tag.x)) | ((a.w ^ tag.x) | (bq.w ^ tag.x));
                flags_ok = none(l < 16 && (fv.x != tag.x || fv.y != tag.y));
                if (none((bad & 0xFFFF) != 0)) break;
                nap();
            }
            STAMP(7);
#define HI2(x, y) __builtin_amdgcn_perm(y, x, 0x07060302u)
            w128(lb + L_KVL + w * 1024 + l * 16, u32x4{HI2(a.x, a.y), HI2(a.z, a.w), HI2(bq.x, bq.y), HI2(bq.z, bq.w)});
            LBAR();
            f32x16 o = {};
#pragma unroll
            for (int s = 0; s < 4; ++s) o = mm32(r128(lb + L_KVL + (hh * 4 + s) * 1024 + l * 16), pq[s], o);
            auto r = __builtin_amdgcn_permlane32_swap(u_(o[12]), u_(o[12]), false, false);
            publish_o(o, __builtin_amdgcn_rcpf(f_(r[1]) + 1e-6f), WS_OB);
        }
        STAMP(8);
    } else {
        const u32 kxo = WS_KX + (t * 16 + head) * 3072 + l * 16;
        if (rg < 3) {
            const u32 ax = lb + L_AX + w * 3072 + l * 16;
            w128(ax, kf0); w128(ax + 1024, kf1); w128(ax + 2048, vf1);
        } else {
            gst(rs, kxo, kf0); gst(rs, kxo + 1024, kf1); gst(rs, kxo + 2048, vf1);
            WAITVM(0);
            gst64(rs, l == 0 ? WS_KF + (t * 16 + head) * 8 : OOB, tag);
        }
        WAITBAR(0);
        STAMP(3);
        if (j == 0) {
            if (tid < 448) w128(lb + L_WQ + tid * 128 + ((7 ^ ((tid & 2) << 1)) << 4), u32x4{gatev, 0, 0, 0});
            LBAR();
        }
        wo_dma(gwoa, L_WOA);
        const f32x16 aq = stage2();
        STAMP(4);
        if (j == 0 && hh == 1) {
            const float gg = __builtin_amdgcn_rcpf(1.f + __builtin_amdgcn_exp2f(aq[12] * -LOG2E));
            __builtin_amdgcn_raw_buffer_store_b32(u_(gg), rs, h ? WS_G + (t * 128 + rg * 32 + tok) * 4 : OOB, 0, SC1);
        }
        u32x4 qf0 = pk8(aq, 0), qf1 = pk8(aq, 1);
        if (h) { qf1.z = 0; qf1.w = 0; }
        u32x4 kp0, kp1, vp;
        bool hasprev = true;
        if (rg > 0) {
            const u32 ax = lb + L_AX + (w - 1) * 3072 + l * 16;
            kp0 = r128(ax); kp1 = r128(ax + 1024); vp = r128(ax + 2048);
        } else if (t > 0) {
            wait_flags(rs, l == 0 ? WS_KF + ((t - 1) * 16 + head) * 8 : OOB, tag);
            kp0 = gld(rs, kxo - 16 * 3072); kp1 = gld(rs, kxo - 16 * 3072 + 1024); vp = gld(rs, kxo - 16 * 3072 + 2048);
        } else {
            kp0 = u32x4{}; kp1 = u32x4{}; vp = u32x4{}; hasprev = false;
        }
        STAMP(5);
        f32x16 so = mm32(kf0, qf0, f32x16{}); so = mm32(kf1, qf1, so);
        f32x16 sp = mm32(kp0, qf0, f32x16{}); sp = mm32(kp1, qf1, sp);
        const u32 om = opq(0u - (tok & 1));
        const u32 base = (tok & ~1u) - 4 * h, basep = hasprev ? base + 32 : 1000u;
        float sv[12];
        const float NEG = -1e30f;
#pragma unroll
        for (int i = 0; i < 8; ++i) { const float v = sel(om, so[2 * i + 1], so[2 * i]); sv[i] = (base - (8 * (i >> 1) + 2 * (i & 1))) <= 14u ? v : NEG; }
#pragma unroll
        for (int i = 4; i < 8; ++i) { const float v = sel(om, sp[2 * i + 1], sp[2 * i]); sv[4 + i] = (basep - (8 * (i >> 1) + 2 * (i & 1))) <= 14u ? v : NEG; }
        float mx = fmaxf(sv[0], sv[1]);
#pragma unroll
        for (int i = 2; i < 12; i += 2) mx = fmaxf(fmaxf(mx, sv[i]), sv[i + 1]);
        const float sc = 0.18898223650461362f * LOG2E;
        mx = xmax32(mx) * -sc;
        float sum = 0.f;
        u32 pd[12];
        const u32 pm = om ^ 0x0000FFFFu;
#pragma unroll
        for (int i = 0; i < 12; ++i) { const float e = __builtin_amdgcn_exp2f(fmaf(sv[i], sc, mx)); sum += e; pd[i] = pk(e, e) & pm; }
        f32x16 o = mm32(vf0, u32x4{pd[0], pd[1], pd[2], pd[3]}, f32x16{});
        o = mm32(vf1, u32x4{pd[4], pd[5], pd[6], pd[7]}, o);
        o = mm32(vp, u32x4{pd[8], pd[9], pd[10], pd[11]}, o);
        publish_o(o, __builtin_amdgcn_rcpf(xadd32(sum)), WS_OA);
        STAMP(6);
        WAITBAR(0);
        gst64(rs, tid == 0 ? WS_FL + (t * 16 + j) * 8 : OOB, tag);
        wo_dma(gwob, L_WOB);
        STAMP(8);
    }

    if (p2) {
        const u32 tt = w & 3, kh = w >> 2;
        if (!flags_ok) wait_flags(rs, flo, tag);
        if (isA) WAITBAR(0);
        STAMP(9);
        const u32 oo = ((u2 * 4 + tt) * 16 + kh * 8) * 1024 + l * 16;
        u32x4 of[8];
#pragma unroll
        for (int e = 0; e < 8; ++e) of[e] = gld(rs, WS_OA + oo + e * 1024);
        const float gg = f_(__builtin_amdgcn_raw_buffer_load_b32(rs, WS_G + (u2 * 64 + tt * 16 + p) * 4, 0, SC1));
        const u32 f0 = 2 * (g & 1) + (p >> 3);
        const u32 wb0 = lb + (224 * kh + 16 * (g >> 1) + 4 * (g & 1) + (p >> 2)) * 128 + ((((p & 3) >> 1) ^ (2 * f0)) << 4) + (p & 1) * 8;
        const u32 wb1 = wb0 + (g == 3 ? 0 : 1024);
        u32 wr0[4], wr1[4];
        auto wbase = [&](u32 img) {
#pragma unroll
            for (int x = 0; x < 4; ++x) { wr0[x] = opq((wb0 + img) ^ (x << 5)); wr1[x] = opq((wb1 + img) ^ (x << 5)); }
        };
        auto wfrag = [&](int e, int nt) {
            const int x = nt ^ (2 * (e & 1));
            return rtr2(wr0[x] + e * 3584, wr1[x] + e * 3584);
        };
        wbase(L_WOA);
        f32x4 ca[4] = {}, cbb[4] = {};
        u32x4 ob[8];
#pragma unroll
        for (int e = 0; e < 8; ++e) {
#pragma unroll
            for (int nt = 0; nt < 4; ++nt) ca[nt] = mm16(wfrag(e, nt), of[e], ca[nt]);
            if (e == 3) {
#pragma unroll
                for (int q = 0; q < 8; ++q) ob[q] = gld(rs, WS_OB + oo + q * 1024);
            }
        }
        STAMP(10);
        u32x4 wf[8][4];
        wbase(L_WOB);
#pragma unroll
        for (int e = 0; e < 8; ++e)
#pragma unroll
            for (int nt = 0; nt < 4; ++nt) wf[e][nt] = wfrag(e, nt);
#pragma nounroll
        for (int it = 0; it < SPIN; ++it) {
            u32 m = 0;
#pragma unroll
            for (int e = 0; e < 8; ++e) m = max(max(m, max(ob[e].x, ob[e].y)), max(ob[e].z, ob[e].w));
            if (none(m == ~0u)) break;
#pragma nounroll
            for (; it < SPIN; ++it) {
                const u32x4 v = gld(rs, WS_OB + oo + 7 * 1024);
                if (none(max(max(v.x, v.y), max(v.z, v.w)) == ~0u)) break;
                nap();
            }
#pragma unroll
            for (int e = 0; e < 8; ++e) ob[e] = gld(rs, WS_OB + oo + e * 1024);
        }
        STAMP(11);
#pragma unroll
        for (int e = 0; e < 8; ++e)
#pragma unroll
            for (int nt = 0; nt < 4; ++nt) cbb[nt] = mm16(wf[e][nt], ob[e], cbb[nt]);
        const u32 xo = lb + L_XO + kh * 16384 + (tt * 16 + p) * 256 + ((g ^ (p & 7)) << 4);
#pragma unroll
        for (int nt = 0; nt < 4; ++nt) {
            const f32x4 r = ca[nt] * gg + cbb[nt] * (1.f - gg);
            w128(xo ^ (nt << 6), __builtin_bit_cast(u32x4, r));
        }
        LBAR();
        const u32 tk = tid >> 3, c8 = tid & 7;
        const u32 xr = lb + L_XO + tk * 256 + (((2 * c8) ^ (tk & 7)) << 4);
        const f32x4 r0 = __builtin_bit_cast(f32x4, r128(xr)) + __builtin_bit_cast(f32x4, r128(xr + 16384));
        const f32x4 r1 = __builtin_bit_cast(f32x4, r128(xr ^ 16)) + __builtin_bit_cast(f32x4, r128((xr ^ 16) + 16384));
        __builtin_amdgcn_raw_buffer_store_b128(u32x4{pk(r0[0], r0[1]), pk(r0[2], r0[3]), pk(r1[0], r1[1]), pk(r1[2], r1[3])},
                                               mk(gout, 2048 * 896), (u2 * 64 + tk) * 896 + cb * 128 + c8 * 16, 0, 16);
        STAMP(12);
    } else if (b == 255 && w < 4) {
        wait_flags(rs, WS_FL + tid * 8, tag);
        asm volatile("s_barrier" ::: "memory");
        gst64(rs, tid == 0 ? WS_E0 : OOB, tag);
    } else if (b == 255) {
        asm volatile("s_barrier" ::: "memory");
    }
    asm volatile(".Lfm_end:" ::: "memory");
}

__global__ void k1(u32* p) { const u32 t = (u32)__builtin_amdgcn_s_memrealtime(); if (threadIdx.x == 0) __hip_atomic_store(p, t, __ATOMIC_RELAXED, __HIP_MEMORY_SCOPE_AGENT); }
__global__ void k3(u32* p) { const u32 t0 = __hip_atomic_load(p, __ATOMIC_RELAXED, __HIP_MEMORY_SCOPE_AGENT); for (int i = 0; i < 100000 && (u32)__builtin_amdgcn_s_memrealtime() - t0 < 4000; ++i) nap(); }
extern "C" void kernel_launch(void* const* d_in, const int* in_sizes, int n_in, void* d_out, int out_size, void* d_ws, size_t ws_size, hipStream_t stream) {
    (void)in_sizes; (void)n_in; (void)out_size; (void)ws_size;
    Args a;
    a.x = d_in[0]; a.wa = d_in[1]; a.wb = d_in[4]; a.wphi = d_in[5]; a.gate = d_in[7]; a.ws = d_ws; a.woa = d_in[2]; a.wob = d_in[6]; a.out = d_out; a.pad = nullptr;
    hipLaunchKernelGGL(k1, dim3(1), dim3(64), 0, stream, (u32*)((char*)d_ws + (17u << 20)));
    hipLaunchKernelGGL(fused, dim3(256), dim3(512), 0, stream, a);
}
```

```cpp
#include <hip/hip_runtime.h>
#include <stdint.h>

typedef unsigned u32;
typedef unsigned long long u64;
typedef u32 u32x4 __attribute__((ext_vector_type(4)));
typedef u32 u32x2 __attribute__((ext_vector_type(2)));
typedef float f32x2 __attribute__((ext_vector_type(2)));
typedef float f32x4 __attribute__((ext_vector_type(4)));
typedef float f32x16 __attribute__((ext_vector_type(16)));
typedef __bf16 bf16x8 __attribute__((ext_vector_type(8)));
typedef __bf16 bf16x2 __attribute__((ext_vector_type(2)));
typedef short s16x4 __attribute__((ext_vector_type(4)));
typedef __amdgpu_buffer_rsrc_t rsrc_t;
#define LDSP(T) __attribute__((address_space(3))) T*
#define DI __device__ __forceinline__

constexpr u32 WS_E0 = 0;
constexpr u32 WS_FL = 1024;
constexpr u32 WS_G = 8192;
constexpr u32 WS_KVR = 16384;
constexpr u32 WS_KX = 256 * 1024;
constexpr u32 WS_P = 1024 * 1024;
constexpr u32 WS_OA = 3 * 1024 * 1024;
constexpr u32 WS_OB = 5 * 1024 * 1024;
constexpr u32 WS_BYTES = 7 * 1024 * 1024;
constexpr u32 SLOT = 24576;
constexpr u32 L_WV = 96 * 1024, L_MISC = 152 * 1024, L_JUNK = 156 * 1024;
constexpr u32 L_PHI = 48 * 1024, L_VP = 80 * 1024, L_AX = 48 * 1024, L_WOA = 96 * 1024, L_WOB = 40 * 1024;
constexpr u32 L_XO = 0, L_KVL = 32 * 1024;
constexpr u32 OOB = 0x80000000u;
constexpr int SPIN = 1 << 15;
constexpr int SC1 = 16;
#define AD1 1500
#define AD2 4000
#define PRIO 1
#define PPOLL 20
#define OBE 5
template <int N> struct ic { static constexpr int value = N; };
constexpr float LOG2E = 1.4426950408889634f;

DI void dma(u32 m0v, u32 voff, rsrc_t rs, u32 soff) {
    asm volatile("s_mov_b32 m0, %0\n\ts_nop 0\n\tbuffer_load_dwordx4 %1, %2, %3 offen lds" ::"s"(m0v), "v"(voff), "s"(rs), "s"(soff) : "memory");
}
DI void dma4(u32 m0v, u32 voff, rsrc_t rs) {
    asm volatile("s_mov_b32 m0, %0\n\ts_nop 0\n\tbuffer_load_dword %1, %2, 0 offen lds" ::"s"(m0v), "v"(voff), "s"(rs) : "memory");
}
template <int N> DI void waitbar() { asm volatile("s_waitcnt vmcnt(%0) lgkmcnt(0)\n\ts_barrier" ::"i"(N) : "memory"); }
#define WAITBAR(N) asm volatile("s_waitcnt vmcnt(" #N ") lgkmcnt(0)\n\ts_barrier" ::: "memory")
#define LBAR() asm volatile("s_waitcnt lgkmcnt(0)\n\ts_barrier" ::: "memory")
#define BAR() asm volatile("s_barrier" ::: "memory")
#define SCHED() __builtin_amdgcn_sched_barrier(0)
DI u32x4 r128(u32 a) { return *(LDSP(const u32x4))(size_t)a; }
DI void w128(u32 a, u32x4 v) { *(LDSP(u32x4))(size_t)a = v; }
DI void w64(u32 a, u32x2 v) { *(LDSP(u32x2))(size_t)a = v; }
DI u32x2 rtr(u32 a) { return __builtin_bit_cast(u32x2, __builtin_amdgcn_ds_read_tr16_b64_v4i16((LDSP(s16x4))(size_t)a)); }
DI u32x4 rtr2(u32 a, u32 b) { u32x2 x = rtr(a), y = rtr(b); return u32x4{x.x, x.y, y.x, y.y}; }
DI f32x16 mm32(u32x4 a, u32x4 b, f32x16 c) { return __builtin_amdgcn_mfma_f32_32x32x16_bf16(__builtin_bit_cast(bf16x8, a), __builtin_bit_cast(bf16x8, b), c, 0, 0, 0); }
DI f32x4 mm16(u32x4 a, u32x4 b, f32x4 c) { return __builtin_amdgcn_mfma_f32_16x16x32_bf16(__builtin_bit_cast(bf16x8, a), __builtin_bit_cast(bf16x8, b), c, 0, 0, 0); }
DI u32 pk(float lo, float hi) { f32x2 v = {lo, hi}; return __builtin_bit_cast(u32, __builtin_convertvector(v, bf16x2)); }
DI u32x4 pk8(const f32x16& a, int s) { return u32x4{pk(a[8 * s], a[8 * s + 1]), pk(a[8 * s + 2], a[8 * s + 3]), pk(a[8 * s + 4], a[8 * s + 5]), pk(a[8 * s + 6], a[8 * s + 7])}; }
DI float f_(u32 x) { return __builtin_bit_cast(float, x); }
DI u32 u_(float x) { return __builtin_bit_cast(u32, x); }
DI float max2(float a, float b) { return __builtin_elementwise_maximum(a, b); }
DI float max3(float a, float b, float c) { return max2(max2(a, b), c); }
DI void pin(f32x16& x) { asm volatile("" : "+v"(x)); }
DI float xmax32(float x) { auto r = __builtin_amdgcn_permlane32_swap(u_(x), u_(x), false, false); return max2(f_(r[0]), f_(r[1])); }
DI float xadd32(float x) { auto r = __builtin_amdgcn_permlane32_swap(u_(x), u_(x), false, false); return f_(r[0]) + f_(r[1]); }
DI u32x4 gld(rsrc_t rs, u32 off) { return __builtin_amdgcn_raw_buffer_load_b128(rs, off, 0, SC1); }
DI void gst(rsrc_t rs, u32 off, u32x4 v) { __builtin_amdgcn_raw_buffer_store_b128(v, rs, off, 0, SC1); }
DI void gst64(rsrc_t rs, u32 off, u32x2 v) { __builtin_amdgcn_raw_buffer_store_b64(v, rs, off, 0, SC1); }
DI void gst32(rsrc_t rs, u32 off, u32 v) { __builtin_amdgcn_raw_buffer_store_b32(v, rs, off, 0, SC1); }
DI u32 opq(u32 x) { asm("" : "+v"(x)); return x; }
DI float sel(u32 m, float a, float b) { return f_((u_(a) & m) | (u_(b) & ~m)); }
DI void nap() { asm volatile("s_sleep 1" ::: "memory"); }
DI bool none(bool bad) { return __builtin_amdgcn_ballot_w64(bad) == 0; }
DI bool unset(u32x4 v) { return max(max(v.x, v.y), max(v.z, v.w)) == ~0u; }
DI rsrc_t mk(const void* p, u32 bytes) { return __builtin_amdgcn_make_buffer_rsrc((void*)p, 0, bytes, 0x00020000); }

template <bool NORM, class H>
DI void phi(const f32x16& p, float keep, u32x4 (&o)[4], H hook) {
    float mx = max2(p[0], p[1]), nm = max2(-p[0], -p[1]);
#pragma unroll
    for (int i = 2; i < 16; i += 2) { mx = max3(p[i], p[i + 1], mx); nm = max3(-p[i], -p[i + 1], nm); if (i == 8) hook(ic<0>{}); }
    hook(ic<1>{});
    {
        auto r = __builtin_amdgcn_permlane32_swap(u_(mx), u_(nm), false, false);
        const float m = max2(f_(r[0]), f_(r[1])) * -LOG2E;
        auto q = __builtin_amdgcn_permlane32_swap(u_(m), u_(m), false, false);
        mx = f_(q[0]); nm = f_(q[1]);
    }
    hook(ic<2>{});
    float a[16], b[16];
    float sa = 0.f, sb = 0.f;
    auto step = [&](auto ii) {
        constexpr int i = 15 - decltype(ii)::value;
        a[i] = __builtin_amdgcn_exp2f(fmaf(p[i], LOG2E, mx)); b[i] = __builtin_amdgcn_exp2f(fmaf(p[i], -LOG2E, nm));
        if (NORM) {
            if constexpr (i == 13) { sa = a[15] + a[14]; sb = b[15] + b[14]; }
            else if constexpr (i == 10) { sa = fmaf(sa, keep, a[11]); sb = fmaf(sb, keep, b[11]); }
            else if constexpr (i < 13) { sa += a[i + 1]; sb += b[i + 1]; }
        }
    };
    step(ic<0>{}); hook(ic<3>{}); step(ic<1>{}); step(ic<2>{}); hook(ic<4>{}); step(ic<3>{}); hook(ic<5>{});
    step(ic<4>{}); hook(ic<6>{}); step(ic<5>{}); step(ic<6>{}); hook(ic<7>{}); step(ic<7>{}); hook(ic<8>{});
    step(ic<8>{}); hook(ic<9>{}); step(ic<9>{}); step(ic<10>{}); hook(ic<10>{}); step(ic<11>{}); hook(ic<11>{});
    step(ic<12>{}); hook(ic<12>{}); step(ic<13>{}); step(ic<14>{}); hook(ic<13>{}); step(ic<15>{}); hook(ic<14>{});
    if (NORM) {
        sa += a[0]; sb += b[0];
        auto r = __builtin_amdgcn_permlane32_swap(u_(sa), u_(sb), false, false);
        const float m = __builtin_amdgcn_rcpf(f_(r[0]) + f_(r[1]));
        auto q = __builtin_amdgcn_permlane32_swap(u_(m), u_(m), false, false);
        hook(ic<15>{});
#pragma unroll
        for (int i = 0; i < 16; ++i) { a[i] *= f_(q[0]); b[i] *= f_(q[1]); }
    }
#pragma unroll
    for (int i = 0; i < 8; ++i) { o[i >> 2][i & 3] = pk(a[2 * i], a[2 * i + 1]); o[2 + (i >> 2)][i & 3] = pk(b[2 * i], b[2 * i + 1]); }
}

#define STAMP(i)

struct Args { const void *x, *wa, *wb, *wphi, *gate; void* ws; const void *woa, *wob; void* out; const void* pad; };
__global__ __launch_bounds__(512, 2) void fused(const Args ar) {
    __shared__ __attribute__((aligned(1024))) char smem[163840];
    STAMP(0);
    const u32 tid = threadIdx.x, l = tid & 63;
    const u32 w = __builtin_amdgcn_readfirstlane(tid >> 6);
    const u32 lb = (u32)(size_t)(LDSP(char))smem;
    const u32 b = blockIdx.x, xcd = b & 7, bi = b >> 3;
    const bool isA = bi >= 16;
    {
        u64 pc; u32 len;
        asm volatile("s_getpc_b64 %0\n.Lfm_begin:\n\ts_mov_b32 %1, .Lfm_end-.Lfm_begin\n\ts_max_i32 %1, %1, 0\n\ts_nop 4" : "=s"(pc), "=s"(len)::"scc");
        if (isA && w < 4) dma4(lb + L_JUNK, tid * 128, mk((const void*)pc, len));
    }
    const void *gx = ar.x, *gwa = ar.wa, *gwb = ar.wb, *gwphi = ar.wphi, *ggate = ar.gate, *gwoa = ar.woa, *gwob = ar.wob;
    void *gws = ar.ws, *gout = ar.out;
    asm volatile("" : "+s"(gx), "+s"(gwa), "+s"(gwb), "+s"(gwphi), "+s"(ggate), "+s"(gws), "+s"(gwoa), "+s"(gwob), "+s"(gout));
    const u32 t = (xcd >> 1) * 4 + ((bi & 15) >> 2), j = (xcd & 1) * 4 + (bi & 3);
    const u32 rg = w & 3, hh = w >> 2, head = 2 * j + hh;
    const u32 tok = l & 31, h = l >> 5, p = l & 15, g = l >> 4;
    const rsrc_t rx = mk(gx, 2048 * 896), rw = mk(isA ? gwa : gwb, 448 * 2688), rs = mk(gws, WS_BYTES);
    auto mktag = [&](u32x2 e) {
        const u64 tg = (((u64)(u32)__builtin_amdgcn_readfirstlane(e.y) << 32) | (u32)__builtin_amdgcn_readfirstlane(e.x)) + 0x9E3779B97F4A7C15ull;
        return u32x2{(u32)tg, (u32)(tg >> 32)};
    };
    const u32 ooff = ((t * 8 + rg * 2 + (tok >> 4)) * 16 + head) * 1024 + h * 256 + p * 16;
    const u32 kxo = WS_KX + (t * 16 + head) * 3072 + l * 16;
    const u32 goff = (j == 0 && hh == 1 && h) ? WS_G + (t * 128 + rg * 32 + tok) * 4 : OOB;
    const u32 un = t >> 1, cg = (t & 1) * 8 + w, gq = cg >> 2;
    constexpr u32 KV2 = 512;
    const u32 kvo = ((l & 12) || w >= 4) ? OOB : WS_KVR + ((2 * j + (un >> 2)) * 4 + ((un >> 1) & 1) * 2 + (gq >> 1)) * 1024 +
                                         (16 * (un & 1) + ((4 * cg + (l & 3)) & 15) + 32 * (gq & 1)) * 16 + g * 4;

    u32x2 tag;
    if (isA) {
        const u32 a = bi & 15, jb = (xcd & 1) * 3;
        const rsrc_t rwb = mk(gwb, 448 * 2688);
        const u32 o1 = a < 8 ? ((t & 12) * 128 + a * 64 + l) * 896 + w * 128
                             : (w * 64 + (a & 7) * 8 + (l >> 3)) * 2688 + (((l >> 2) & 1) * 7 + 7 + jb + (l & 3)) * 128;
        dma4(lb + L_JUNK, w < 7 ? o1 : OOB, a < 8 ? rx : rwb);
        const u64 clk0 = __builtin_amdgcn_s_memtime();
        const u32x4 ones = {~0u, ~0u, ~0u, ~0u};
        gst32(rs, kvo, ~0u); gst32(rs, kvo + KV2, ~0u);
        gst(rs, WS_OB + ooff, ones); gst(rs, WS_OB + ooff + 512, ones);
        gst(rs, WS_OA + ooff, ones); gst(rs, WS_OA + ooff + 512, ones);
        gst(rs, rg < 3 ? kxo + rg * 1024 : OOB, ones);
        gst32(rs, goff, ~0u);
#pragma nounroll
        for (int it = 0; it < 256 && __builtin_amdgcn_s_memtime() - clk0 < AD1; ++it) nap();
        const u32 m2 = a >> 2;
        dma4(lb + L_JUNK, w < 7 ? (w * 64 + (a & 3) * 16 + (l >> 2)) * 2688 + ((m2 == 3 ? 0 : m2) * 7 + jb + (l & 3)) * 128 : OOB, m2 ? rw : rwb);
#pragma nounroll
        for (int it = 0; it < 256 && __builtin_amdgcn_s_memtime() - clk0 < AD2; ++it) nap();
    }

    const u32 vx0 = (t * 128 + w * 16 + (l >> 3)) * 896 + (((l & 7) ^ g) << 4);
    const u32 vx1 = (vx0 + 8 * 896) ^ 64;
    const u32 wsrc = (l & 7) ^ ((g & 1) << 2);
    const u32 vw = wsrc == 7 ? OOB : (w * 8 + (l >> 3)) * 2688 + j * 112 + wsrc * 16;
    auto chunk1 = [&](u32 c, int q) {
        const u32 sl = lb + (c & 3) * SLOT;
        if (q == 0) dma(sl + w * 2048, vx0, rx, c * 128);
        if (q == 1) dma(sl + w * 2048 + 1024, vx1, rx, c * 128);
        if (q == 2) dma(sl + 16384 + w * 1024, vw, rw, c * (64 * 2688) + 896);
    };
    auto chunk = [&](u32 c) { chunk1(c, 0); chunk1(c, 1); chunk1(c, 2); };
    auto wv_dma = [&](u32 q) { dma(lb + L_WV + q * 8192 + w * 1024, vw, rw, q * (64 * 2688) + 1792); };
    auto wq_dma = [&](u32 q) { dma(lb + (q < 6 ? q * 8192 : L_MISC) + w * 1024, vw, rw, q * (64 * 2688)); };
    chunk(0);
    BAR();
    STAMP(1);
    u32 gatev; u32x2 e0v;
    asm volatile("buffer_load_dwordx2 %0, off, %1, 0 sc1" : "=v"(e0v) : "s"(rs) : "memory");
    asm volatile("buffer_load_ushort %0, %1, %2, 0 offen" : "=v"(gatev) : "v"(tid * 2), "s"(mk(ggate, isA ? 896 : 0)) : "memory");
    dma(lb + L_MISC + (w & 3) * 1024, (w & 1) ? (l < 34 ? l * 16 + 1024 : OOB) : l * 16, mk(gwphi, isA ? 0 : 16 * 1568), (2 * j + ((w >> 1) & 1)) * 1568);
    chunk(1);
    wv_dma(0);
    BAR();
    STAMP(14);
    chunk(2);
    STAMP(13);

    const u32 xa = lb + (rg * 32 + tok) * 128 + ((h ^ ((tok >> 1) & 7)) << 4);
    const u32 wa = lb + (8 * h + (p >> 2)) * 128 + ((56 * hh + 32 * (g & 1) + 8 * (p & 3)) ^ (((p >> 3) & 1) << 6));
    const u32 xah = opq(xa + 2 * SLOT), wah = opq(wa + 2 * SLOT), wvb = opq(wa + L_WV);
    u32x4 xf[28], wphi[2];
    f32x16 ak = {}, av = {};
    u32x4 wk[2][4], wv[4];
    auto rd = [&](int c, int s) {
        const u32 sl = (c & 1) * SLOT, xb = (c & 2) ? xah : xa, wb = (c & 2) ? wah : wa;
        xf[4 * c + s] = r128((xb ^ (32 * s)) + sl);
        wk[c & 1][s] = rtr2(wb + sl + 16384 + s * 2048, wb + sl + 16384 + s * 2048 + 512);
    };
    auto rdv = [&](int ks) { if (ks < 28) wv[ks & 3] = rtr2(wvb + ks * 2048, wvb + ks * 2048 + 512); };
    auto mk_ = [&](int c, int s) { if (c >= 0) { ak = mm32(wk[c & 1][s], xf[4 * c + s], ak); pin(ak); } SCHED(); };
    auto mv_ = [&](int ks) { if (ks >= 0) { av = mm32(xf[ks], wv[ks & 3], av); pin(av); } SCHED(); };
    auto issue = [&](int c, int q) {
        if (c < 4) { if (q < 3) chunk1(c + 3, q); else if (!(c & 1)) wv_dma(1 + c / 2); }
        else if (c == 4) { if (q < 3) wv_dma(3 + q); }
        else if (c == 5) { if (q == 0) wv_dma(6); if (q == 1) wq_dma(6); }
        else if (q < 3) wq_dma(q);
        if (c == 1 && q == 3) {
            asm volatile("" : "+v"(gatev), "+v"(e0v));
            tag = mktag(e0v);
            gst64(rs, tid == 0 ? WS_FL + (isA ? 0 : 1024) + (t * 8 + j) * 8 : OOB, tag);
        }
        SCHED();
    };
    auto iter = [&](auto cc) {
        constexpr int c = decltype(cc)::value;
        constexpr int W[7] = {10, 8, 8, 9, 8, 7, 5};
        waitbar<W[c]>();
        STAMP(16 + c);
        rd(c, 0); rd(c, 1);
        SCHED();
        mk_(c - 1, 1);
        issue(c, 0);
        rd(c, 2); rd(c, 3);
        SCHED();
        mk_(c - 1, 2);
        issue(c, 1);
        mk_(c - 1, 3);
        mv_(2 * c - 6);
        issue(c, 2);
        mv_(2 * c - 5);
        if (c == 1) {
            const u32 fq = 4 * (g & 1) + (p & 3);
            const u32 pa = lb + L_MISC + hh * 2048 + (4 * h + (p >> 2)) * 56 + (fq == 7 ? 6 : fq) * 8;
            wphi[0] = rtr2(pa, pa + 8 * 56); wphi[1] = rtr2(pa + 16 * 56, pa + 24 * 56);
        }
        if (c >= 2) { rdv(2 * c - 4); rdv(2 * c - 3); }
        if (c == 6) { rdv(10); rdv(11); }
        SCHED();
        mk_(c, 0);
        issue(c, 3);
    };
    iter(ic<0>{}); iter(ic<1>{}); iter(ic<2>{}); iter(ic<3>{}); iter(ic<4>{}); iter(ic<5>{}); iter(ic<6>{});
    mk_(6, 1); mk_(6, 2); mk_(6, 3);
    mv_(8); mv_(9);
    const u32x4 kf0 = pk8(ak, 0), kf1 = pk8(ak, 1);
    STAMP(2);

    const u32 i2 = (bi + 28) & 31;
    const bool p2 = i2 < 28;
    const u32 u2 = xcd * 4 + i2 / 7, cb = i2 % 7;
    const u32 vwo = (rg * 8 + (l >> 3)) * 896 + cb * 128 + (((l & 7) ^ (g << 1)) << 4);
    const rsrc_t roa = mk(gwoa, p2 ? 448 * 896 : 0), rob = mk(gwob, p2 ? 448 * 896 : 0);
    auto wo_dma = [&](rsrc_t ro, u32 dst, u32 q0, u32 q1) {
        if (hh) {
#pragma unroll
            for (u32 q = q0; q < q1; ++q) dma(lb + dst + q * 4096 + rg * 1024, vwo, ro, q * (32 * 896));
        }
    };
    auto flags_bad = [&](u32x4 v) { return ((v.x ^ tag.x) | (v.y ^ tag.y) | (v.z ^ tag.x) | (v.w ^ tag.y)) != 0; };
    auto wait_flags = [&](u32x4 v, u32 off) {
#pragma nounroll
        for (int it = 0; it < SPIN && !none(flags_bad(v)); ++it) { nap(); v = gld(rs, off); }
        asm volatile("" ::: "memory");
    };
    auto stage2 = [&](u32 qb, auto early) {
        f32x16 aq = {};
        const u32 wq = opq(wa), wq6 = opq(wa + L_MISC);
        u32x4 f[3];
        auto rq = [&](int ks) { if (ks < 28) f[ks % 3] = ks < 24 ? rtr2(wq + ks * 2048, wq + ks * 2048 + 512) : rtr2(wq6 + (ks - 24) * 2048, wq6 + (ks - 24) * 2048 + 512); };
        if (PRIO && !hh) __builtin_amdgcn_s_setprio(1);
        wo_dma(roa, L_WOA, 0, 14);
        rq(0); rq(1);
#pragma unroll
        for (int ks = 0; ks < 28; ++ks) {
            rq(ks + 2);
            aq = mm32(f[ks % 3], xf[ks], aq);
            SCHED();
            if (ks % 3 == 1 && qb + (ks / 3) * 2 < 14) wo_dma(rob, L_WOB, qb + (ks / 3) * 2, qb + (ks / 3) * 2 + 2);
            if (ks == PPOLL) early();
            SCHED();
        }
        return aq;
    };
    auto publish_o = [&](const f32x16& o, u32 base) {
        u32x4 o0 = pk8(o, 0), o1 = pk8(o, 1);
        if (h) { o1.z = 0; o1.w = 0; }
        gst(rs, base + ooff, o0); gst(rs, base + ooff + 512, o1);
    };
    const u32 tt = w & 3, kh = w >> 2;
    const u32 oo = ((u2 * 4 + tt) * 16 + kh * 8) * 1024 + l * 16;
    u32x4 of[8];
    u32 ggv;
    auto load_oa = [&](u32 from) {
#pragma unroll
        for (u32 e = 0; e < 8; ++e) of[e] = gld(mk(gws, p2 && e >= from ? WS_BYTES : 0), WS_OA + oo + e * 1024);
        ggv = __builtin_amdgcn_raw_buffer_load_b32(rs, WS_G + (u2 * 64 + tt * 16 + p) * 4, 0, SC1);
    };

    if (isA) {
        mv_(10); mv_(11);
        if (rg == 3) { gst(rs, kxo, kf0); gst(rs, kxo + 1024, kf1); WAITBAR(6); } else WAITBAR(4);
        rdv(12); rdv(13);
#pragma unroll
        for (int n = 0; n < 16; ++n) {
            rdv(14 + n); mv_(12 + n);
            if (n < 3) wq_dma(3 + n);
            SCHED();
        }
        const u32x4 vf0 = pk8(av, 0), vf1 = pk8(av, 1);
        if (rg < 3) {
            const u32 ax = lb + L_AX + w * 3072 + l * 16;
            w128(ax, kf0); w128(ax + 1024, kf1); w128(ax + 2048, vf1);
            WAITBAR(0);
        } else {
            gst(rs, kxo + 2048, vf1);
            WAITBAR(1);
        }
        STAMP(3);
        if (j == 0) {
            if (tid < 448) w128(lb + (tid < 384 ? tid * 128 : L_MISC - 384 * 128 + tid * 128) + ((7 ^ ((tid & 2) << 1)) << 4), u32x4{gatev, 0, 0, 0});
            LBAR();
        }
        u32x4 kp0, kp1, vp, fv;
        const rsrc_t rsp = mk(gws, rg == 0 && t > 0 ? WS_BYTES : 0);
        const u32 kpo = kxo - 16 * 3072;
        const f32x16 aq = stage2(8, [&]() { fv = gld(rs, WS_FL + l * 16); kp0 = gld(rsp, kpo); kp1 = gld(rsp, kpo + 1024); vp = gld(rsp, kpo + 2048); });
        STAMP(4);
        if (!none(flags_bad(fv))) { wait_flags(fv, WS_FL + l * 16); kp0.x = rg == 0 && t > 0 ? ~0u : 0u; }
        if (rg > 0) {
            const u32 ax = lb + L_AX + (w - 1) * 3072 + l * 16;
            kp0 = r128(ax); kp1 = r128(ax + 1024); vp = r128(ax + 2048);
        } else {
#pragma nounroll
            for (int it = 0; it < SPIN && !none(unset(kp0) || unset(kp1) || unset(vp)); ++it) {
                asm volatile("" ::: "memory");
                kp0 = gld(rsp, kpo); kp1 = gld(rsp, kpo + 1024); vp = gld(rsp, kpo + 2048);
            }
        }
        if (hh) LBAR();
        wo_dma(rob, L_WOB, 0, 8);
        STAMP(5);
        gst32(rs, goff, u_(__builtin_amdgcn_rcpf(1.f + __builtin_amdgcn_exp2f(aq[12] * -LOG2E))));
        u32x4 qf0 = pk8(aq, 0), qf1 = pk8(aq, 1);
        if (h) { qf1.z = 0; qf1.w = 0; }
        f32x16 so = mm32(kf0, qf0, f32x16{}); so = mm32(kf1, qf1, so);
        f32x16 sp = mm32(kp0, qf0, f32x16{}); sp = mm32(kp1, qf1, sp);
        const u32 om = opq(0u - (tok & 1));
        const u32 base = (tok & ~1u) - 4 * h, basep = (rg > 0 || t > 0) ? base + 32 : 1000u;
        float sv[12];
        const float NEG = -1e30f;
#pragma unroll
        for (int i = 0; i < 8; ++i) { const float v = sel(om, so[2 * i + 1], so[2 * i]); sv[i] = (base - (8 * (i >> 1) + 2 * (i & 1))) <= 14u ? v : NEG; }
#pragma unroll
        for (int i = 4; i < 8; ++i) { const float v = sel(om, sp[2 * i + 1], sp[2 * i]); sv[4 + i] = (basep - (8 * (i >> 1) + 2 * (i & 1))) <= 14u ? v : NEG; }
        float mx = max2(sv[0], sv[1]);
#pragma unroll
        for (int i = 2; i < 12; i += 2) mx = max3(sv[i], sv[i + 1], mx);
        const float sc = 0.18898223650461362f * LOG2E;
        mx = xmax32(mx) * -sc;
        float sum = 0.f;
        u32 pd[12];
        const u32 pm = om ^ 0x0000FFFFu;
#pragma unroll
        for (int i = 0; i < 12; ++i) { const float e = __builtin_amdgcn_exp2f(fmaf(sv[i], sc, mx)); sum += e; pd[i] = pk(e, e) & pm; }
        f32x16 o = mm32(vf0, u32x4{pd[0], pd[1], pd[2], pd[3]}, f32x16{});
        o = mm32(vf1, u32x4{pd[4], pd[5], pd[6], pd[7]}, o);
        o = mm32(vp, u32x4{pd[8], pd[9], pd[10], pd[11]}, o);
        publish_o(o * __builtin_amdgcn_rcpf(xadd32(sum)), WS_OA);
        STAMP(8);
        if (!hh) LBAR();
        load_oa(0);
    } else {
        const float keep = h ? 0.f : 1.f;
        {
            f32x16 pp = mm32(wphi[0], kf0, f32x16{});
            pp = mm32(wphi[1], kf1, pp);
            pin(pp);
            mv_(10); mv_(11);
            WAITBAR(4);
            STAMP(23);
            rdv(12); rdv(13);
            SCHED();
            u32x4 ph[4];
            phi<true>(pp, keep, ph, [&](auto nn) {
                constexpr int n = decltype(nn)::value;
                SCHED();
                rdv(14 + n); mv_(12 + n);
                if (n >= 1 && n <= 3) wq_dma(2 + n);
                SCHED();
            });
            const u32 F = (((tok >> 1) & 3) << 2) | (((tok >> 3) & 1) << 1) | (tok & 1);
            const u32 pw = lb + L_PHI + w * 4096 + tok * 128 + ((h ^ F) << 3);
#pragma unroll
            for (int s = 0; s < 4; ++s) {
                const u32 cu = 8 * (s >> 1) + 2 * (s & 1);
                w64(pw ^ (cu << 3), u32x2{ph[s].x, ph[s].y});
                w64(pw ^ ((cu + 4) << 3), u32x2{ph[s].z, ph[s].w});
            }
            u32x4 vf0 = pk8(av, 0), vf1 = pk8(av, 1);
            if (tok == 28) { vf0 = u32x4{0x3F803F80u, 0x3F803F80u, 0x3F803F80u, 0x3F803F80u}; vf1 = vf0; }
            w128(lb + L_VP + w * 2048 + l * 16, vf0); w128(lb + L_VP + w * 2048 + 1024 + l * 16, vf1);
        }
        LBAR();
        STAMP(3);
        {
            const u32 sign = rg >> 1, dt = rg & 1;
            const u32 F = ((2 * (g & 1) + (p >> 3)) << 2) | ((p >> 2) & 1);
            const u32 pr = lb + L_PHI + hh * 16384 + (16 * (g >> 1) + 4 * (g & 1) + (p >> 2)) * 128 + (((8 * sign + (p & 3)) ^ F) << 3);
            const u32 vr = lb + L_VP + hh * 8192 + g * 512 + (16 * dt + p) * 16;
            f32x4 c0 = {}, c1 = {};
#pragma unroll
            for (int r = 0; r < 4; ++r) {
                const u32x4 vv = r128(vr + r * 2048);
                c0 = mm16(rtr2(pr + r * 4096, (pr ^ 16) + r * 4096 + 1024), vv, c0);
                c1 = mm16(rtr2((pr ^ 32) + r * 4096, (pr ^ 48) + r * 4096 + 1024), vv, c1);
            }
            const u32 t0 = tag.x & 0xFFF, t1 = (tag.x >> 12) & 0xFFF, t2 = tag.y & 0xFFF, t3 = (tag.y >> 12) & 0xFFF;
            const u32 po = WS_P + (j * 8 + w) * 32768 + t * 1024 + l * 16;
#define TG(x, tt) ((u_(x) & 0xFFFFF000u) | (tt))
            gst(rs, po, u32x4{TG(c0[0], t0), TG(c0[1], t1), TG(c0[2], t2), TG(c0[3], t3)});
            gst(rs, po + 16384, u32x4{TG(c1[0], t0), TG(c1[1], t1), TG(c1[2], t2), TG(c1[3], t3)});
        }
        WAITBAR(2);
        STAMP(4);
        const u32 po = WS_P + (j * 8 + un) * 32768 + (l >> 2) * 1024 + (4 * cg + (l & 3)) * 16;
        const rsrc_t rsr = mk(gws, hh ? 0 : WS_BYTES);
        u32x4 pa[4], fv;
        auto ldp = [&]() { pa[0] = gld(rsr, po); pa[1] = gld(rsr, po + 16384); pa[2] = gld(rsr, po + 256); pa[3] = gld(rsr, po + 16384 + 256); };
        const f32x16 aq = stage2(2, [&]() { fv = gld(rsr, WS_FL + l * 16); ldp(); });
        STAMP(5);
        if (!hh) {
            wait_flags(fv, WS_FL + l * 16);
#pragma nounroll
            for (int it = 0; it < SPIN; ++it) {
                const u32x4 d = (pa[0] ^ pa[1]) | (pa[0] ^ pa[2]) | (pa[0] ^ pa[3]) | (pa[0] ^ u32x4{tag.x, tag.x >> 12, tag.y, tag.y >> 12});
                if (none(((d.x | d.y | d.z | d.w) & 0xFFF) != 0)) break;
                asm volatile("" ::: "memory");
                ldp();
            }
            STAMP(6);
#pragma unroll
            for (int z = 0; z < 2; ++z) {
                const u32 mb = (z && (t & 1)) ? 0u : 0xFFFFF000u, cbit = (z && (t & 1)) ? 0u : 0x800u;
                float uu[4];
#pragma unroll
                for (int i = 0; i < 4; ++i) {
                    auto r = __builtin_amdgcn_permlane32_swap((pa[2 * z][i] & 0xFFFFF000u) | 0x800u, (pa[2 * z + 1][i] & mb) | cbit, false, false);
                    uu[i] = f_(r[0]) + f_(r[1]);
                }
                float ww[2];
#pragma unroll
                for (int i = 0; i < 2; ++i) {
                    auto r = __builtin_amdgcn_permlane16_swap(u_(uu[i]), u_(uu[i + 2]), false, false);
                    float y = f_(r[0]) + f_(r[1]);
                    y += f_(__builtin_amdgcn_update_dpp(0, u_(y), 0x128, 0xf, 0xf, false));
                    y += f_(__builtin_amdgcn_update_dpp(0, u_(y), 0x124, 0xf, 0xf, false));
                    ww[i] = y;
                }
                gst32(rs, kvo + z * KV2, pk(ww[0], ww[1]));
            }
        } else {
            LBAR();
            wo_dma(rob, L_WOB, 0, 2);
        }
        u32x4 pq[4];
        {
            f32x16 pp = mm32(wphi[0], pk8(aq, 0), f32x16{});
            pp = mm32(wphi[1], pk8(aq, 1), pp);
            phi<false>(pp, keep, pq, [&](auto) {});
            asm volatile("" : "+v"(pq[0]), "+v"(pq[1]), "+v"(pq[2]), "+v"(pq[3]));
        }
        STAMP(15);
        {
            if (!hh) {
                LBAR();
                const u32 ko = WS_KVR + (2 * j * 4 + rg) * 1024 + l * 16;
                u32x4 kv[2];
#pragma nounroll
                for (int it = 0; it < SPIN; ++it) {
                    kv[0] = gld(rs, ko); kv[1] = gld(rs, ko + 4096);
                    if (none(unset(kv[0]) || unset(kv[1]))) break;
                    asm volatile("" ::: "memory");
                }
                STAMP(7);
                if (tok == 29) {
                    const u32 z = (h && (rg & 1)) ? 0u : 0x3F803F80u;
                    kv[0] = kv[1] = u32x4{0x3F803F80u, 0x3F803F80u, z, z};
                }
                w128(lb + L_KVL + w * 1024 + l * 16, kv[0]); w128(lb + L_KVL + 4096 + w * 1024 + l * 16, kv[1]);
            }
            WAITBAR(0);
            const u32 kl = lb + L_KVL + hh * 4096 + l * 16;
            f32x16 op = mm32(r128(kl), pq[0], f32x16{}), on = mm32(r128(kl + 2048), pq[2], f32x16{});
            op = mm32(r128(kl + 1024), pq[1], op); on = mm32(r128(kl + 3072), pq[3], on);
            const float rd_ = __builtin_amdgcn_rcpf(fmaf(op[12], on[13], fmaf(on[12], op[13], 1e-6f * op[13] * on[13])));
            auto r = __builtin_amdgcn_permlane32_swap(u_(on[13] * rd_), u_(op[13] * rd_), false, false);
            auto r2 = __builtin_amdgcn_permlane32_swap(r[1], r[1], false, false);
            publish_o(op * f_(r2[0]) + on * f_(r2[1]), WS_OB);
        }
        STAMP(8);
        load_oa(0);
    }

    if (p2) {
        const u32 f0 = 2 * (g & 1) + (p >> 3);
        const u32 wb0 = lb + (224 * kh + 16 * (g >> 1) + 4 * (g & 1) + (p >> 2)) * 128 + ((((p & 3) >> 1) ^ (2 * f0)) << 4) + (p & 1) * 8;
        const u32 wb1 = wb0 + (g == 3 ? 0 : 1024);
        u32x4 wf[8][4];
        auto wfrags = [&](u32 img) {
            u32 wr0[4], wr1[4];
#pragma unroll
            for (int x = 0; x < 4; ++x) { wr0[x] = opq((wb0 + img) ^ (x << 5)); wr1[x] = opq((wb1 + img) ^ (x << 5)); }
#pragma unroll
            for (int e = 0; e < 8; ++e)
#pragma unroll
                for (int nt = 0; nt < 4; ++nt) { const int x = nt ^ (2 * (e & 1)); wf[e][nt] = rtr2(wr0[x] + e * 3584, wr1[x] + e * 3584); }
        };
        f32x4 ca[4] = {}, cbb[4] = {};
        u32x4 ob[8];
        auto load_ob = [&](u32 from) {
#pragma unroll
            for (u32 e = 0; e < 8; ++e) ob[e] = gld(mk(gws, e >= from ? WS_BYTES : 0), WS_OB + oo + e * 1024);
        };
        auto pass = [&](const u32x4 (&f)[8], f32x4 (&c)[4]) -> u32 {
#pragma unroll
            for (u32 e = 0; e < 8; ++e) {
                if (!none(unset(f[e]))) return e;
#pragma unroll
                for (int nt = 0; nt < 4; ++nt) c[nt] = mm16(wf[e][nt], f[e], c[nt]);
            }
            return 8;
        };
        if (PRIO) __builtin_amdgcn_s_setprio(0);
        wfrags(L_WOA);
        STAMP(9);
#pragma nounroll
        for (int it = 0; it < SPIN; ++it) {
            const u32 e = pass(of, ca);
            if (e == 8 && none(ggv == ~0u)) break;
            asm volatile("" ::: "memory");
            load_oa(e);
        }
        STAMP(10);
        if (isA) {
            LBAR();
#pragma nounroll
            for (int it = 0; it < SPIN; ++it) {
                const u32 v = __builtin_amdgcn_raw_buffer_load_b32(rs, l < 8 ? WS_OB + oo + l * 1008 : OOB, 0, SC1);
                if (!none(l < 8 && v != ~0u)) break;
                nap();
            }
        }
        load_ob(0);
        wfrags(L_WOB);
        const float gg = f_(ggv);
#pragma unroll
        for (int nt = 0; nt < 4; ++nt) ca[nt] *= gg;
#pragma nounroll
        for (int it = 0; it < SPIN; ++it) {
            const u32 e = pass(ob, cbb);
            if (e == 8) break;
            asm volatile("" ::: "memory");
            load_ob(e);
        }
        STAMP(11);
        const u32 xo = lb + L_XO + kh * 16384 + (tt * 16 + p) * 256 + ((g ^ (p & 7)) << 4);
#pragma unroll
        for (int nt = 0; nt < 4; ++nt) w128(xo ^ (nt << 6), __builtin_bit_cast(u32x4, ca[nt] + cbb[nt] * (1.f - gg)));
        LBAR();
        const u32 tk = tid >> 3, c8 = tid & 7;
        const u32 xr = lb + L_XO + tk * 256 + (((2 * c8) ^ (tk & 7)) << 4);
        const f32x4 r0 = __builtin_bit_cast(f32x4, r128(xr)) + __builtin_bit_cast(f32x4, r128(xr + 16384));
        const f32x4 r1 = __builtin_bit_cast(f32x4, r128(xr ^ 16)) + __builtin_bit_cast(f32x4, r128((xr ^ 16) + 16384));
        gst(mk(gout, 2048 * 896), (u2 * 64 + tk) * 896 + cb * 128 + c8 * 16, u32x4{pk(r0[0], r0[1]), pk(r0[2], r0[3]), pk(r1[0], r1[1]), pk(r1[2], r1[3])});
        STAMP(12);
    } else if (b == 31 && w == 0) {
        wait_flags(gld(rs, WS_FL + 1024 + l * 16), WS_FL + 1024 + l * 16);
        gst64(rs, l == 0 ? WS_E0 : OOB, tag);
    }
    asm volatile(".Lfm_end:" ::: "memory");
}

extern "C" void kernel_launch(void* const* d_in, const int* in_sizes, int n_in, void* d_out, int out_size, void* d_ws, size_t ws_size, hipStream_t stream) {
    (void)in_sizes; (void)n_in; (void)out_size; (void)ws_size;
    Args a;
    a.x = d_in[0]; a.wa = d_in[1]; a.wb = d_in[4]; a.wphi = d_in[5]; a.gate = d_in[7]; a.ws = d_ws; a.woa = d_in[2]; a.wob = d_in[6]; a.out = d_out; a.pad = nullptr;
    hipLaunchKernelGGL(fused, dim3(256), dim3(512), 0, stream, a);
}
```

```cpp
#include <hip/hip_runtime.h>
#include <stdint.h>

typedef unsigned u32;
typedef unsigned long long u64;
typedef u32 u32x4 __attribute__((ext_vector_type(4)));
typedef u32 u32x2 __attribute__((ext_vector_type(2)));
typedef float f32x2 __attribute__((ext_vector_type(2)));
typedef float f32x4 __attribute__((ext_vector_type(4)));
typedef float f32x16 __attribute__((ext_vector_type(16)));
typedef __bf16 bf16x8 __attribute__((ext_vector_type(8)));
typedef __bf16 bf16x2 __attribute__((ext_vector_type(2)));
typedef short s16x4 __attribute__((ext_vector_type(4)));
typedef __amdgpu_buffer_rsrc_t rsrc_t;
#define LDSP(T) __attribute__((address_space(3))) T*
#define DI __device__ __forceinline__

constexpr u32 WS_E0 = 0;
constexpr u32 WS_FL = 1024;
constexpr u32 WS_KVR = 16384;
constexpr u32 WS_KX = 256 * 1024;
constexpr u32 WS_P = 1024 * 1024;
constexpr u32 WS_OA = 3 * 1024 * 1024;
constexpr u32 WS_OB = 5 * 1024 * 1024;
constexpr u32 WS_BYTES = 7 * 1024 * 1024;
constexpr u32 slot(bool A, int c) { return A ? (c & 3) * 32768 : 16384 + ((6 - c) % 6) * 24576; }
constexpr u32 sbase(bool A, int c) { return A ? (c & 2) * 32768 : c == 1 || c == 2 ? slot(false, 2) : c == 3 || c == 4 ? slot(false, 4) : 0; }
constexpr u32 wofs(int ks) { return (6 - (ks >> 2)) * 8192 + (ks & 3) * 2048; }
constexpr u32 L_WV = 104 * 1024, L_MISC = 0, L_PHI = 0, L_VP = 32 * 1024, L_AX = 0;
constexpr u32 OOB = 0x80000000u, ONES = 0x3F803F80u;
constexpr int SPIN = 1 << 15;
constexpr int SC1 = 16;
#define AD2 0
#define W1X 1
#define VB0 2
#define PRIO 1
#define PPOLL 9
#define KPOLL 20
template <int N> struct ic { static constexpr int value = N; };
constexpr float LOG2E = 1.4426950408889634f;

DI void dma(u32 m0v, u32 voff, rsrc_t rs, u32 soff) {
    asm volatile("s_mov_b32 m0, %0\n\ts_nop 0\n\tbuffer_load_dwordx4 %1, %2, %3 offen lds" ::"s"(m0v), "v"(voff), "s"(rs), "s"(soff) : "memory");
}
DI void dmax(u32 m0v, u32 voff, rsrc_t rs, u32 soff, u64 mask) {
    asm volatile("s_mov_b32 m0, %0\n\ts_mov_b64 exec, %4\n\tbuffer_load_dwordx4 %1, %2, %3 offen lds\n\ts_mov_b64 exec, -1" ::"s"(m0v), "v"(voff), "s"(rs), "s"(soff), "s"(mask) : "memory");
}
DI u32 touch(u32 voff, rsrc_t rs) {
    u32 r; asm volatile("buffer_load_dword %0, %1, %2, 0 offen" : "=v"(r) : "v"(voff), "s"(rs) : "memory"); return r;
}
DI void drop(u32 r) { asm volatile("" ::"v"(r)); }
template <int N> DI void waitbar() { asm volatile("s_waitcnt vmcnt(%0) lgkmcnt(0)\n\ts_barrier" ::"i"(N) : "memory"); }
#define WAITBAR(N) asm volatile("s_waitcnt vmcnt(" #N ") lgkmcnt(0)\n\ts_barrier" ::: "memory")
#define VBAR(N) asm volatile("s_waitcnt vmcnt(" #N ")\n\ts_barrier" ::: "memory")
#define VM0() __builtin_amdgcn_s_waitcnt(0x0F70)
#define LBAR() asm volatile("s_waitcnt lgkmcnt(0)\n\ts_barrier" ::: "memory")
#define BAR() asm volatile("s_barrier" ::: "memory")
#define SCHED() __builtin_amdgcn_sched_barrier(0)
DI u32x4 r128(u32 a) { return *(LDSP(const u32x4))(size_t)a; }
DI void w128(u32 a, u32x4 v) { *(LDSP(u32x4))(size_t)a = v; }
DI void w64(u32 a, u32x2 v) { *(LDSP(u32x2))(size_t)a = v; }
DI u32x2 rtr(u32 a) { return __builtin_bit_cast(u32x2, __builtin_amdgcn_ds_read_tr16_b64_v4i16((LDSP(s16x4))(size_t)a)); }
DI u32x4 rtr2(u32 a, u32 b) { u32x2 x = rtr(a), y = rtr(b); return u32x4{x.x, x.y, y.x, y.y}; }
DI f32x16 mm32(u32x4 a, u32x4 b, f32x16 c) { return __builtin_amdgcn_mfma_f32_32x32x16_bf16(__builtin_bit_cast(bf16x8, a), __builtin_bit_cast(bf16x8, b), c, 0, 0, 0); }
DI f32x4 mm16(u32x4 a, u32x4 b, f32x4 c) { return __builtin_amdgcn_mfma_f32_16x16x32_bf16(__builtin_bit_cast(bf16x8, a), __builtin_bit_cast(bf16x8, b), c, 0, 0, 0); }
DI u32 pk(float lo, float hi) { f32x2 v = {lo, hi}; return __builtin_bit_cast(u32, __builtin_convertvector(v, bf16x2)); }
DI u32x4 pk8(const f32x16& a, int s) { return u32x4{pk(a[8 * s], a[8 * s + 1]), pk(a[8 * s + 2], a[8 * s + 3]), pk(a[8 * s + 4], a[8 * s + 5]), pk(a[8 * s + 6], a[8 * s + 7])}; }
DI float f_(u32 x) { return __builtin_bit_cast(float, x); }
DI u32 u_(float x) { return __builtin_bit_cast(u32, x); }
DI float max2(float a, float b) { return __builtin_elementwise_maximum(a, b); }
DI float max3(float a, float b, float c) { return max2(max2(a, b), c); }
DI float mul1(float a, float b) { float r; asm("v_mul_f32 %0, %1, %2" : "=v"(r) : "v"(a), "v"(b)); return r; }
DI u32 bfi(u32 m, u32 a, u32 b) { return (a & m) | (b & ~m); }
DI void pin(f32x16& x) { asm volatile("" : "+v"(x)); }
DI float xmax32(float x) { auto r = __builtin_amdgcn_permlane32_swap(u_(x), u_(x), false, false); return max2(f_(r[0]), f_(r[1])); }
DI float xadd32(float x) { auto r = __builtin_amdgcn_permlane32_swap(u_(x), u_(x), false, false); return f_(r[0]) + f_(r[1]); }
DI u32x4 gld(rsrc_t rs, u32 off) { return __builtin_amdgcn_raw_buffer_load_b128(rs, off, 0, SC1); }
DI void gst(rsrc_t rs, u32 off, u32x4 v) { __builtin_amdgcn_raw_buffer_store_b128(v, rs, off, 0, SC1); }
DI void gst64(rsrc_t rs, u32 off, u32x2 v) { __builtin_amdgcn_raw_buffer_store_b64(v, rs, off, 0, SC1); }
DI void gst32(rsrc_t rs, u32 off, u32 v) { __builtin_amdgcn_raw_buffer_store_b32(v, rs, off, 0, SC1); }
DI u32 opq(u32 x) { asm("" : "+v"(x)); return x; }
DI float sel(u32 m, float a, float b) { return f_((u_(a) & m) | (u_(b) & ~m)); }
DI void nap() { asm volatile("s_sleep 1" ::: "memory"); }
DI bool none(bool bad) { return __builtin_amdgcn_ballot_w64(bad) == 0; }
DI u32x4 max(u32x4 a, u32x4 b) { return __builtin_elementwise_max(a, b); }
DI bool unset(u32x4 v) { return max(max(v.x, v.y), max(v.z, v.w)) == ~0u; }
DI rsrc_t mk(const void* p, u32 bytes) { return __builtin_amdgcn_make_buffer_rsrc((void*)p, 0, bytes, 0x00020000); }

template <bool NORM, class H, class E>
DI void phi(const f32x16& p, float keep, H hook, E emit) {
    float mx = max2(p[0], p[1]), nm = max2(-p[0], -p[1]);
    auto mstep = [&](int i) { mx = max3(p[i], p[i + 1], mx); nm = max3(-p[i], -p[i + 1], nm); };
    mstep(2); hook(ic<0>{}); mstep(4); mstep(6); hook(ic<1>{}); mstep(8); mstep(10); hook(ic<2>{}); mstep(12); mstep(14); hook(ic<3>{});
    {
        auto r = __builtin_amdgcn_permlane32_swap(u_(mx), u_(nm), false, false);
        const float m = max2(f_(r[0]), f_(r[1])) * -LOG2E;
        auto q = __builtin_amdgcn_permlane32_swap(u_(m), u_(m), false, false);
        mx = f_(q[0]); nm = f_(q[1]);
    }
    hook(ic<4>{});
    float a[16], b[16];
    float sa = 0.f, sb = 0.f;
    auto step = [&](auto ii) {
        constexpr int i = 15 - decltype(ii)::value;
        a[i] = __builtin_amdgcn_exp2f(fmaf(p[i], LOG2E, mx)); b[i] = __builtin_amdgcn_exp2f(fmaf(p[i], -LOG2E, nm));
        if (NORM) {
            if constexpr (i == 13) { sa = a[15] + a[14]; sb = b[15] + b[14]; }
            else if constexpr (i == 10) { sa = fmaf(sa, keep, a[11]); sb = fmaf(sb, keep, b[11]); }
            else if constexpr (i < 13) { sa += a[i + 1]; sb += b[i + 1]; }
        }
        hook(ic<20 - i>{});
    };
    step(ic<0>{}); step(ic<1>{}); step(ic<2>{}); step(ic<3>{}); step(ic<4>{}); step(ic<5>{}); step(ic<6>{}); step(ic<7>{});
    step(ic<8>{}); step(ic<9>{}); step(ic<10>{}); step(ic<11>{}); step(ic<12>{}); step(ic<13>{}); step(ic<14>{}); step(ic<15>{});
    if (NORM) {
        sa += a[0]; sb += b[0];
        auto r = __builtin_amdgcn_permlane32_swap(u_(sa), u_(sb), false, false);
        const float m = __builtin_amdgcn_rcpf(f_(r[0]) + f_(r[1]));
        auto q = __builtin_amdgcn_permlane32_swap(u_(m), u_(m), false, false);
        sa = f_(q[0]); sb = f_(q[1]);
    }
    auto out = [&](auto ss) {
        constexpr int s = decltype(ss)::value;
        hook(ic<21 + s>{});
        float* e = (s & 2) ? b + 8 * (s & 1) : a + 8 * (s & 1);
        if (NORM) {
#pragma unroll
            for (int i = 0; i < 8; ++i) e[i] = mul1(e[i], (s & 2) ? sb : sa);
        }
        emit(ss, u32x4{pk(e[0], e[1]), pk(e[2], e[3]), pk(e[4], e[5]), pk(e[6], e[7])});
    };
    out(ic<0>{}); out(ic<1>{}); out(ic<2>{}); out(ic<3>{});
}

#define STAMP(i)
#define STAMPB(i)

struct Args { const void *x, *wa, *wb, *wphi, *gate; void* ws; const void *woa, *wob; void* out; const void* pad; };
template <bool isA>
DI void body(const Args& ar, const u32 lb) {
    STAMP(0);
    const u32 tid = threadIdx.x, l = tid & 63;
    const u32 w = __builtin_amdgcn_readfirstlane(tid >> 6);
    const u32 b = blockIdx.x, xcd = b & 7, bi = b >> 3;
    const void *gx = ar.x, *gwa = ar.wa, *gwb = ar.wb, *gwphi = ar.wphi, *ggate = ar.gate, *gwoa = ar.woa, *gwob = ar.wob;
    void *gws = ar.ws, *gout = ar.out;
    asm volatile("" : "+s"(gx), "+s"(gwa), "+s"(gwb), "+s"(gwphi), "+s"(ggate), "+s"(gws), "+s"(gwoa), "+s"(gwob), "+s"(gout));
    const u32 t = (xcd >> 1) * 4 + ((bi & 15) >> 2), j = (xcd & 1) * 4 + (bi & 3);
    const u32 rg = w & 3, hh = w >> 2, head = 2 * j + hh;
    const u32 tok = l & 31, h = l >> 5, p = l & 15, g = l >> 4;
    const rsrc_t rx = mk(gx, 2048 * 896), rw = mk(isA ? gwa : gwb, 448 * 2688), rs = mk(gws, WS_BYTES);
    constexpr u32 L_WQ = isA ? 104 * 1024 : 48 * 1024, L_WOA = isA ? 48 * 1024 : 104 * 1024, L_WOB = isA ? 104 * 1024 : 0, L_XO = isA ? 0 : 56 * 1024;
    const u32 a16 = bi & 15, jb = (xcd & 1) * 3;
    const u32 un = t >> 1, cg = (t & 1) * 8 + w, gq = cg >> 2;
    constexpr u32 KV2 = 512;
    const u32 kvd = 16 * (un & 1) + ((4 * cg + (l & 3)) & 15);
    const u32 kvo = WS_KVR + ((2 * j + (un >> 2)) * 4 + ((un >> 1) & 1) * 2 + (gq >> 1)) * 1024 + (kvd + 32 * (gq & 1)) * 16 + g * 4;
    u32 jk0 = 0, jk1 = 0, jk2 = 0;
    if constexpr (isA) {
        const rsrc_t rwb = mk(gwb, 448 * 2688);
        const bool sp = w == 0 && a16 < 3;
        const u32 o1 = sp ? (a16 < 2 ? (xcd & 1) * 12544 + a16 * 8192 : 0) + l * 128
                     : a16 < 8 ? ((t & 12) * 128 + a16 * 64 + l) * 896 + w * 128
                               : (w * 64 + (a16 & 7) * 8 + (l >> 3)) * 2688 + (((l >> 2) & 1) * 7 + 7 + jb + (l & 3)) * 128;
        jk0 = touch(sp || (w < 7 && (w >= W1X || (a16 >= 8 && (l & 4)))) ? o1 : OOB, sp ? (a16 < 2 ? mk(gwphi, 16 * 1568) : mk(ggate, 896)) : a16 < 8 ? rx : rwb);
        const u64 clk0 = __builtin_amdgcn_s_memtime();
        BAR();
        jk1 = touch(l < 32 ? (w < 7 ? (w * 64 + a16 * 4 + (l >> 3)) * 2688 + (((l >> 2) & 1) * 7 + 7 + jb + (l & 3)) * 128 : OOB) : OOB, rw);
        BAR();
        jk2 = touch(w < 2 && (w * 16 + (l >> 2)) < 28 ? (a16 * 28 + w * 16 + (l >> 2)) * 2688 + (jb + (l & 3)) * 128 : OOB, rwb);
#pragma nounroll
        for (int it = 0; it < 256 && __builtin_amdgcn_s_memtime() - clk0 < AD2; ++it) nap();
        BAR();
    } else gst32(rs, (l & 8) || w >= 4 ? OOB : kvo + (l & 4) * (KV2 / 4), ~0u);

    const u32 vx0 = (t * 128 + w * 16 + (l >> 3)) * 896 + (((l & 7) ^ g) << 4);
    const u32 vx1 = (vx0 + 8 * 896) ^ 64;
    const u32 wsrc = (l & 7) ^ ((g & 1) << 2);
    const u32 vw = wsrc == 7 ? OOB : (w * 8 + (l >> 3)) * 2688 + j * 112 + wsrc * 16;
    auto chunk1 = [&](int c, int q) {
        const u32 sl = lb + slot(isA, c);
        if (q == 0) dma(sl + w * 2048, vx0, rx, c * 128);
        if (q == 1) dma(sl + w * 2048 + 1024, vx1, rx, c * 128);
        if (q == 2) dma(sl + 16384 + w * 1024, vw, rw, c * (64 * 2688) + 896);
        if (q == 3 && isA) dma(sl + 24576 + w * 1024, vw, rw, c * (64 * 2688) + 1792);
    };
    auto chunk = [&](int c) { chunk1(c, 0); chunk1(c, 1); chunk1(c, 2); chunk1(c, 3); };
    auto wv_dma = [&](int q) { dma(lb + L_WV + wofs(4 * q) + w * 1024, vw, rw, q * (64 * 2688) + 1792); };
    auto wq_dma = [&](int q) { dmax(lb + L_WQ + wofs(4 * q) + w * 1024, vw, rw, q * (64 * 2688), 0xF7F77F7FF7F77F7Full); };
    chunk(0);
    BAR();
    STAMP(1);
    chunk(1);
    BAR();
    STAMP(14);
    chunk(2);
    STAMP(13);
    u64 tg = *(const __attribute__((address_space(4))) u64*)(size_t)gws + 0x9E3779B97F4A7C15ull;
    asm volatile("" : "+s"(tg));
    const u32x2 tag = {(u32)tg, (u32)(tg >> 32)};
    u32 gatev = 0;
    auto misc = [&]() {
        if (!isA && !hh) dma(lb + L_MISC + w * 1024, (w & 1) ? (l < 34 ? l * 16 + 1024 : OOB) : l * 16, mk(gwphi, 16 * 1568), (2 * j + (w >> 1)) * 1568);
        else gatev = touch(tid * 4 - 1024, mk(ggate, 896));
    };
    auto gate_col = [&]() {
        asm volatile("" : "+v"(gatev));
        if (tid - 256 < 224) {
            const u32 r = 2 * tid - 512, ga = lb + L_WQ + (6 - (r >> 6)) * 8192 + (r & 63) * 128 + ((7 ^ ((r & 2) << 1)) << 4);
            w128(ga, u32x4{gatev & 0xFFFFu, 0, 0, 0}); w128(ga + 128, u32x4{gatev >> 16, 0, 0, 0});
        }
    };
    auto flag = [&]() { gst64(rs, tid == 0 ? WS_FL + b * 8 : OOB, tag); };

    const u32 xa = lb + (rg * 32 + tok) * 128 + ((h ^ ((tok >> 1) & 7)) << 4);
    const u32 wrow = lb + (8 * h + (p >> 2)) * 128, wcol = 32 * (g & 1) + 8 * (p & 3), wsw = ((p >> 3) & 1) << 6;
    const u32 wa = wrow + ((56 * hh + wcol) ^ wsw);
    u32x4 xf[28], wphi[2];
    f32x16 ak = {}, av = {};
    u32x4 wk[2][4], wv[4];
    u32 xb, wb;
    auto rd = [&](int c, int s) {
        const u32 sl = slot(isA, c) - sbase(isA, c);
        xf[4 * c + s] = r128((xb ^ (32 * s)) + sl);
        wk[isA ? 0 : c & 1][s] = rtr2(wb + sl + 16384 + s * 2048, wb + sl + 16384 + s * 2048 + 512);
        if (isA) wv[s] = rtr2(wb + sl + 24576 + s * 2048, wb + sl + 24576 + s * 2048 + 512);
    };
    auto mk_ = [&](int c, int s) { if (c >= 0) { ak = mm32(wk[isA ? 0 : c & 1][s], xf[4 * c + s], ak); pin(ak); } SCHED(); };
    auto mv_ = [&](int ks) { if (ks >= 0) { av = mm32(xf[ks], wv[ks & 3], av); pin(av); } SCHED(); };
    auto issue = [&](int c, int q) {
        if (c < 4) { chunk1(c + 3, q); if (!isA && c == 3 && q == 3) misc(); }
        else if (isA) {
            const int n = 4 * (c - 4) + q;
            if (n == 0) misc(); else if (n < 8) wq_dma(n - 1);
            else if (n == 8) jk0 = touch(l < 25 ? (a16 * 196 + w * 25 + l) * 128 : OOB, mk(gwob, 448 * 896));
        } else if (q < 3) {
            const int n = 3 * (c - 4) + q;
            if (n < 7) wv_dma(n);
            else if (n == 7) jk0 = touch(l < 16 && w < 7 ? (a16 * 28 + w * 4 + (l >> 2)) * 2688 + (jb + (l & 3)) * 128 : OOB, mk(gwa, 448 * 2688));
            else jk1 = touch(l < 25 ? (a16 * 196 + w * 25 + l) * 128 : OOB, mk(gwoa, 448 * 896));
        }
        SCHED();
    };
    auto iter = [&](auto cc) {
        constexpr int c = decltype(cc)::value;
        constexpr int W[7] = {6, 6, 6, 6, 7, 7, 6};
        if (c == 0 || sbase(isA, c) != sbase(isA, c - 1)) { xb = opq(xa + sbase(isA, c)); wb = opq(wa + sbase(isA, c)); }
        waitbar<isA ? 8 : W[c]>();
        if (c == 0) { drop(jk0); drop(jk1); drop(jk2); }
        STAMP(16 + c);
        rd(c, 0); rd(c, 1);
        SCHED();
        if constexpr (isA) {
            mk_(c - 1, 2); issue(c, 0); mv_(4 * c - 2); issue(c, 1);
            mk_(c - 1, 3); issue(c, 2); mv_(4 * c - 1); issue(c, 3);
            rd(c, 2); rd(c, 3);
            SCHED();
            mk_(c, 0); mv_(4 * c); mk_(c, 1); mv_(4 * c + 1);
        } else {
            mk_(c - 1, 1);
            issue(c, 0);
            rd(c, 2); rd(c, 3);
            SCHED();
            mk_(c - 1, 2);
            issue(c, 1);
            mk_(c - 1, 3);
            issue(c, 2);
            if (c == 6) {
                const u32 fq = 4 * (g & 1) + (p & 3);
                const u32 pa = lb + L_MISC + hh * 2048 + (4 * h + (p >> 2)) * 56 + (fq == 7 ? 6 : fq) * 8;
                wphi[0] = rtr2(pa, pa + 8 * 56); wphi[1] = rtr2(pa + 16 * 56, pa + 24 * 56);
                gate_col();
            }
            SCHED();
            mk_(c, 0);
            issue(c, 3);
        }
    };
    iter(ic<0>{}); iter(ic<1>{}); iter(ic<2>{}); iter(ic<3>{}); iter(ic<4>{}); iter(ic<5>{}); iter(ic<6>{});
    if (!isA) mk_(6, 1);
    mk_(6, 2); mk_(6, 3);
    if (isA) { mv_(26); mv_(27); }
    const u32x4 kf0 = pk8(ak, 0), kf1 = pk8(ak, 1);
    STAMP(2);

    const u32 i2 = (bi + 28) & 31;
    const bool p2 = i2 < 28;
    const u32 u2 = xcd * 4 + i2 / 7, cb = i2 % 7;
    const u32 vwo = (rg * 8 + (l >> 3)) * 896 + cb * 128 + (((l & 7) ^ (g << 1)) << 4);
    const rsrc_t roa = mk(gwoa, p2 ? 448 * 896 : 0), rob = mk(gwob, p2 ? 448 * 896 : 0);
    auto wob_dma = [&]() {
#pragma unroll
        for (int n = 0; n < 7; ++n) dma(lb + L_WOB + n * 8192 + w * 1024, vwo, rob, (2 * n + hh) * (32 * 896));
    };
    auto stage2 = [&](auto early) {
        f32x16 aq = {};
        const u32 wq = opq(wrow + L_WQ + ((wcol == 56 ? 112 : 56 * hh + wcol) ^ wsw));
        u32x4 f[3];
        auto rq = [&](int ks) { if (ks < 28) f[ks % 3] = rtr2(wq + wofs(ks), wq + wofs(ks) + 512); };
        if (PRIO && !hh) __builtin_amdgcn_s_setprio(1);
        if (hh) {
#pragma unroll
            for (int n = 0; n < 14; ++n) dma(lb + L_WOA + n * 4096 + rg * 1024, vwo, roa, n * (32 * 896));
        }
        rq(0); rq(1);
#pragma unroll
        for (int ks = 0; ks < 28; ++ks) {
            rq(ks + 2);
            aq = mm32(f[ks % 3], xf[ks], aq);
            pin(aq);
            SCHED();
            if (ks == KPOLL) early();
            SCHED();
        }
        return aq;
    };
    auto tagged = [&](u32x4& a, u32x4& c) {
        auto r = __builtin_amdgcn_permlane32_swap(c.z, c.w, false, false);
        c.z = r[0]; c.w = bfi(0xFFFF0000u, a.w, tag.y); a.w = bfi(0x0000FFFFu, a.w, tag.y);
    };
    const u32 ooff = ((t * 8 + rg * 2 + (tok >> 4)) * 16 + head) * 1024 + h * 256 + p * 16;
    auto publish_o = [&](const f32x16& o, u32 base) {
        u32x4 o0 = pk8(o, 0), o1 = pk8(o, 1);
        tagged(o0, o1);
        gst(rs, base + ooff, o0); gst(rs, base + ooff + 512, o1);
    };
    const u32 th = w & 1, kq = w >> 1;
    const u32 oo = ((u2 * 4 + th * 2) * 16 + kq * 4) * 1024 + l * 16;
    u32x4 of[8];
    auto load_o = [&](u32 base, u32 from) {
#pragma unroll
        for (u32 e = 0; e < 8; ++e) of[e] = gld(mk(gws, e >= from ? WS_BYTES : 0), base + oo + (e >> 1) * 1024 + (e & 1) * 16384);
    };
    auto wfrags = [&](u32x4 (&wf)[4][4], u32 img) {
        const u32 i = p >> 2, r0 = 16 * (g >> 1) + 4 * (g & 1) + i, r1 = g < 2 ? r0 + 8 : i < 2 ? 24 + 2 * (g & 1) + i : 8 + 4 * (g & 1) + i;
        const u32 c = lb + img + 112 * 128 * kq + (p & 1) * 8;
        const u32 wb0 = c + r0 * 128 + ((((p & 3) >> 1) ^ (r0 & 6)) << 4), wb1 = c + r1 * 128 + ((((p & 3) >> 1) ^ (r1 & 6)) << 4);
        u32 wr0[4], wr1[4];
#pragma unroll
        for (int x = 0; x < 4; ++x) { wr0[x] = opq(wb0 ^ (x << 5)); wr1[x] = opq(wb1 ^ (x << 5)); }
#pragma unroll
        for (int e = 0; e < 4; ++e)
#pragma unroll
            for (int nt = 0; nt < 4; ++nt) { const int x = nt ^ (2 * (e & 1)); wf[e][nt] = rtr2(wr0[x] + e * 3584, wr1[x] + e * 3584); }
    };

    auto passes = [&](f32x4 (&cc)[2][4], const u32x4 (&wf)[4][4], u32 base) {
        const u32 tm = g < 2 ? 0xFFFF0000u : 0x0000FFFFu, ts = tag.y & tm;
        auto pass = [&](u32 from) -> u32 {
#pragma unroll
            for (u32 e = 0; e < 8; ++e) {
                if (e < from) continue;
                u32x4 f = of[e];
                f.w ^= ts;
                if (!none((f.w & tm) != 0)) return e;
#pragma unroll
                for (int nt = 0; nt < 4; ++nt) cc[e & 1][nt] = mm16(wf[e >> 1][nt], f, cc[e & 1][nt]);
            }
            return 8;
        };
        u32 e = 0;
#pragma nounroll
        for (int it = 0;; ++it) {
            e = pass(e);
            if (e == 8 || it == SPIN) break;
            asm volatile("" ::: "memory");
            load_o(base, e);
        }
    };
    auto leave = [&]() {
        if (!isA) VM0();
    };
    auto finish = [&](const f32x4 (&cc)[2][4]) {
        STAMP(11);
        const u32 xo = lb + L_XO + kq * 16384 + (th * 32 + p) * 256 + ((g ^ (p & 7)) << 4);
#pragma unroll
        for (int nt = 0; nt < 4; ++nt) { w128(xo ^ (nt << 6), __builtin_bit_cast(u32x4, cc[0][nt])); w128((xo ^ (nt << 6)) + 4096, __builtin_bit_cast(u32x4, cc[1][nt])); }
        LBAR();
        const u32 tk = tid >> 3, c8 = tid & 7;
        const u32 xr = lb + L_XO + tk * 256 + (((2 * c8) ^ (tk & 7)) << 4);
        auto rsum = [&](u32 a) { auto f = [&](u32 o) { return __builtin_bit_cast(f32x4, r128(a + o)); }; return (f(0) + f(16384)) + (f(32768) + f(49152)); };
        const f32x4 r0 = rsum(xr), r1 = rsum(xr ^ 16);
        gst(mk(gout, 2048 * 896), (u2 * 64 + tk) * 896 + cb * 128 + c8 * 16, u32x4{pk(r0[0], r0[1]), pk(r0[2], r0[3]), pk(r1[0], r1[1]), pk(r1[2], r1[3])});
        STAMP(12);
    };

    if constexpr (isA) {
        const u32 kxo = WS_KX + (t * 16 + head) * 3072 + l * 16;
        const u32x4 vf0 = pk8(av, 0), vf1 = pk8(av, 1);
        const u32 mlo = h ? 0u : 0x0000FFFFu, mhi = h ? 0xFFFF0000u : 0u;
        asm volatile("s_waitcnt vmcnt(1)" ::: "memory");
        gate_col();
        if (rg < 3) {
            const u32 ax = lb + L_AX + w * 3072 + l * 16;
            w128(ax, kf0); w128(ax + 1024, kf1); w128(ax + 2048, vf1);
        } else {
            u32x4 a = kf0, c = kf1;
            tagged(a, c); gst(rs, kxo, a); gst(rs, kxo + 1024, c);
            auto r = __builtin_amdgcn_permlane32_swap(vf1.w, vf1.x, false, false);
            gst(rs, kxo + 2048, u32x4{bfi(mlo, tag.y, r[1]), vf1.y, vf1.z, bfi(mhi, tag.y, vf1.w)});
        }
        if (w == 0) flag();
        LBAR();
        STAMP(3);
        u32x4 kp0, kp1, vp;
        const bool kx = rg == 0 && t > 0;
        const u32 kpo = kxo - 16 * 3072;
        const rsrc_t rk = mk(gws, kx ? WS_BYTES : 0);
        const f32x16 aq = stage2([&]() { kp0 = gld(rk, kpo); kp1 = gld(rk, kpo + 1024); vp = gld(rk, kpo + 2048); });
        STAMP(4);
        if (kx) {
#pragma nounroll
            for (int it = 0;; ++it) {
                if (none(((bfi(0xFFFF0000u, kp0.w, kp1.w) ^ tag.y) | ((bfi(mlo, vp.x, vp.w) ^ tag.y) & (mlo | mhi))) != 0) | (it == SPIN)) break;
                asm volatile("" ::: "memory");
                kp0 = gld(rs, kpo); kp1 = gld(rs, kpo + 1024); vp = gld(rs, kpo + 2048);
            }
            VM0();
            kp0.w = bfi(0x0000FFFFu, kp0.w, kp1.w);
            kp1.w = __builtin_amdgcn_permlane32_swap(kp1.z, kp1.z, false, false)[1];
            vp.x &= ~mlo;
            vp.w = bfi(mhi, __builtin_amdgcn_permlane32_swap(vp.x, vp.x, false, false)[0], vp.w);
        } else {
            const u32 ax = lb + L_AX + (rg ? w - 1 : w) * 3072 + l * 16;
            kp0 = r128(ax); kp1 = r128(ax + 1024); vp = r128(ax + 2048);
        }
        STAMP(5);
        const float ge = 1.f + __builtin_amdgcn_exp2f(aq[12] * -LOG2E);
        u32x4 qf0 = pk8(aq, 0), qf1 = pk8(aq, 1);
        if (h) { qf1.z = 0; qf1.w = 0; }
        f32x16 so = mm32(kf0, qf0, f32x16{}); so = mm32(kf1, qf1, so);
        f32x16 sp = mm32(kp0, qf0, f32x16{}); sp = mm32(kp1, qf1, sp);
        const u32 om = opq(0u - (tok & 1));
        const u32 base = (tok & ~1u) - 4 * h, basep = (rg > 0 || t > 0) ? base + 32 : 1000u;
        float sv[12];
        const float NEG = -1e30f;
#pragma unroll
        for (int i = 0; i < 8; ++i) { const float v = sel(om, so[2 * i + 1], so[2 * i]); sv[i] = (base - (8 * (i >> 1) + 2 * (i & 1))) <= 14u ? v : NEG; }
#pragma unroll
        for (int i = 4; i < 8; ++i) { const float v = sel(om, sp[2 * i + 1], sp[2 * i]); sv[4 + i] = (basep - (8 * (i >> 1) + 2 * (i & 1))) <= 14u ? v : NEG; }
        float mx = max2(sv[0], sv[1]);
#pragma unroll
        for (int i = 2; i < 12; i += 2) mx = max3(sv[i], sv[i + 1], mx);
        const float sc = 0.18898223650461362f * LOG2E;
        mx = xmax32(mx) * -sc;
        float sum = 0.f;
        u32 pd[12];
        const u32 pm = om ^ 0x0000FFFFu;
#pragma unroll
        for (int i = 0; i < 12; ++i) { const float e = __builtin_amdgcn_exp2f(fmaf(sv[i], sc, mx)); sum += e; pd[i] = pk(e, e) & pm; }
        f32x16 o = mm32(vf0, u32x4{pd[0], pd[1], pd[2], pd[3]}, f32x16{});
        o = mm32(vf1, u32x4{pd[4], pd[5], pd[6], pd[7]}, o);
        o = mm32(vp, u32x4{pd[8], pd[9], pd[10], pd[11]}, o);
        const u32 cf = u_(__builtin_amdgcn_rcpf(xadd32(sum) * ge));
        publish_o(o * f_(__builtin_amdgcn_permlane32_swap(cf, cf, false, false)[1]), WS_OA);
        STAMP(8);
        VBAR(2);
        drop(jk0);
        wob_dma();
        load_o(WS_OA, 0);
        u32x4 wf[4][4];
        f32x4 cc[2][4] = {};
        wfrags(wf, L_WOA);
        if (PRIO) __builtin_amdgcn_s_setprio(0);
        STAMP(9);
        passes(cc, wf, WS_OA);
        STAMP(10);
        WAITBAR(0);
        wfrags(wf, L_WOB);
#pragma nounroll
        for (int it = 0; it < SPIN; ++it) {
            const u32 v = __builtin_amdgcn_raw_buffer_load_b32(rs, l < 4 ? WS_KVR + (kq * 4 + l) * 4096 : OOB, 0, SC1);
            if (none(v == ~0u)) break;
            nap();
        }
        load_o(WS_OB, 0);
        passes(cc, wf, WS_OB);
        finish(cc);
        leave();
    } else {
        const float keep = h ? 0.f : 1.f;
        {
            f32x16 pp = mm32(wphi[0], kf0, f32x16{});
            pp = mm32(wphi[1], kf1, pp);
            pin(pp);
            STAMP(23);
            const u32 wvb = opq(wa + L_WV);
            auto rdv = [&](int ks) { if (ks < 28) wv[ks & 3] = rtr2(wvb + wofs(ks), wvb + wofs(ks) + 512); };
            const u32 F = (((tok >> 1) & 3) << 2) | (((tok >> 3) & 1) << 1) | (tok & 1);
            const u32 pw = lb + L_PHI + w * 4096 + tok * 128 + ((h ^ F) << 3);
            SCHED();
            phi<true>(pp, keep, [&](auto nn) {
                constexpr int n = decltype(nn)::value;
                constexpr int k0 = n <= VB0 ? 0 : (n - VB0 - 1) * 28 / (24 - VB0), k1 = n <= VB0 ? 0 : (n - VB0) * 28 / (24 - VB0);
                SCHED();
                if (n == VB0) { STAMPB(1); VBAR(6); STAMPB(14); rdv(0); rdv(1); }
#pragma unroll
                for (int k = k0; k < k1; ++k) {
                    if (k == 10) { STAMPB(13); VBAR(2); STAMPB(16); }
                    if (k == 20) STAMPB(17);
                    rdv(k + 2); mv_(k);
                    if (k >= 10 && k < 24 && !(k & 1)) wq_dma((k - 10) / 2);
                }
                SCHED();
            }, [&](auto ss, u32x4 v) {
                constexpr u32 s = decltype(ss)::value, cu = 8 * (s >> 1) + 2 * (s & 1);
                w64(pw ^ (cu << 3), u32x2{v.x, v.y});
                w64(pw ^ ((cu + 4) << 3), u32x2{v.z, v.w});
            });
            STAMPB(18);
            u32x4 vf0 = pk8(av, 0), vf1 = pk8(av, 1);
            if (tok == 28) { vf0 = u32x4{ONES, ONES, ONES, ONES}; vf1 = vf0; }
            w128(lb + L_VP + w * 2048 + l * 16, vf0); w128(lb + L_VP + w * 2048 + 1024 + l * 16, vf1);
        }
        STAMPB(19);
        LBAR();
        STAMP(3);
        {
            const u32 sign = rg >> 1, dt = rg & 1;
            const u32 F = ((2 * (g & 1) + (p >> 3)) << 2) | ((p >> 2) & 1);
            const u32 pr = lb + L_PHI + hh * 16384 + (16 * (g >> 1) + 4 * (g & 1) + (p >> 2)) * 128 + (((8 * sign + (p & 3)) ^ F) << 3);
            const u32 vr = lb + L_VP + hh * 8192 + g * 512 + (16 * dt + p) * 16;
            f32x4 c0 = {}, c1 = {};
#pragma unroll
            for (int r = 0; r < 4; ++r) {
                const u32x4 vv = r128(vr + r * 2048);
                c0 = mm16(rtr2(pr + r * 4096, (pr ^ 16) + r * 4096 + 1024), vv, c0);
                c1 = mm16(rtr2((pr ^ 32) + r * 4096, (pr ^ 48) + r * 4096 + 1024), vv, c1);
            }
            const u32 po = WS_P + (j * 8 + w) * 32768 + t * 1024 + l * 16;
#define TG(x, tt) bfi(0xFFFFF000u, u_(x), tt)
            gst(rs, po, u32x4{TG(c0[0], tag.x), TG(c0[1], tag.x >> 12), TG(c0[2], tag.y), TG(c0[3], tag.y >> 12)});
            gst(rs, po + 16384, u32x4{TG(c1[0], tag.x), TG(c1[1], tag.x >> 12), TG(c1[2], tag.y), TG(c1[3], tag.y >> 12)});
        }
        flag();
        STAMPB(20);
        WAITBAR(3);
        drop(jk0); drop(jk1);
        STAMP(4);
        const f32x16 aq = stage2([&]() {});
        STAMP(5);
        const u32 po = WS_P + (j * 8 + un) * 32768 + (l >> 2) * 1024 + (4 * cg + (l & 3)) * 16;
        const rsrc_t rsr = mk(gws, hh ? 0 : WS_BYTES);
        u32x4 pa[4];
        u32 tgm = hh ? 0u : 0xFFFu;
        asm("" : "+s"(tgm));
        auto ldp = [&]() { pa[0] = gld(rsr, po); pa[1] = gld(rsr, po + 16384); pa[2] = gld(rsr, po + 256); pa[3] = gld(rsr, po + 16384 + 256); };
        u32x4 pq[4];
        float ge = 1.f + __builtin_amdgcn_exp2f(aq[12] * LOG2E);
        {
            f32x16 pp = mm32(wphi[0], pk8(aq, 0), f32x16{});
            pp = mm32(wphi[1], pk8(aq, 1), pp);
            phi<false>(pp, keep, [&](auto nn) { if (decltype(nn)::value == PPOLL) { SCHED(); ldp(); SCHED(); } }, [&](auto ss, u32x4 v) { pq[decltype(ss)::value] = v; });
            asm volatile("" : "+v"(pq[0]), "+v"(pq[1]), "+v"(pq[2]), "+v"(pq[3]), "+v"(ge));
        }
        STAMP(15);
#pragma nounroll
        for (int it = 0;; ++it) {
            const u32x4 d = (pa[0] ^ pa[1]) | (pa[0] ^ pa[2]) | (pa[0] ^ pa[3]) | (pa[0] ^ u32x4{tag.x, tag.x >> 12, tag.y, tag.y >> 12});
            if (none(((d.x | d.y | d.z | d.w) & tgm) != 0) | (it == SPIN)) break;
            asm volatile("" ::: "memory");
            ldp();
        }
        VM0();
        STAMP(6);
        if (!hh) {
#pragma unroll
            for (int z = 0; z < 2; ++z) {
                const bool pad = z && (t & 1);
                const u32 mb = pad ? 0u : 0xFFFFF000u, cbit = pad ? 0u : 0x800u;
                float uu[4];
#pragma unroll
                for (int i = 0; i < 4; ++i) {
                    auto r = __builtin_amdgcn_permlane32_swap((pa[2 * z][i] & 0xFFFFF000u) | 0x800u, (pa[2 * z + 1][i] & mb) | cbit, false, false);
                    uu[i] = f_(r[0]) + f_(r[1]);
                }
                float ww[2];
#pragma unroll
                for (int i = 0; i < 2; ++i) {
                    auto r = __builtin_amdgcn_permlane16_swap(u_(uu[i]), u_(uu[i + 2]), false, false);
                    float y = f_(r[0]) + f_(r[1]);
                    y += f_(__builtin_amdgcn_update_dpp(0, u_(y), 0x128, 0xf, 0xf, false));
                    y += f_(__builtin_amdgcn_update_dpp(0, u_(y), 0x124, 0xf, 0xf, false));
                    ww[i] = y;
                }
                gst32(rs, (l & 12) ? OOB : kvo + z * KV2, kvd == 29 ? (pad && h ? 0u : ONES) : pk(ww[0], ww[1]));
            }
        }
        BAR();
        wob_dma();
        u32x4 wfa[4][4], wfb[4][4];
        {
            const u32 ko = WS_KVR + head * 4096 + l * 16;
            u32x4 kv[4];
            auto ldk = [&]() { kv[0] = gld(rs, ko); kv[1] = gld(rs, ko + 1024); kv[2] = gld(rs, ko + 2048); kv[3] = gld(rs, ko + 3072); };
            ldk();
            wfrags(wfa, L_WOA);
#pragma nounroll
            for (int it = 0;; ++it) {
                if (none(unset(max(max(kv[0], kv[1]), max(kv[2], kv[3])))) | (it == SPIN)) break;
                asm volatile("" ::: "memory");
                ldk();
            }
            STAMP(7);
            f32x16 op = mm32(kv[0], pq[0], f32x16{}), on = mm32(kv[2], pq[2], f32x16{});
            op = mm32(kv[1], pq[1], op); on = mm32(kv[3], pq[3], on);
            const float rd_ = __builtin_amdgcn_rcpf(fmaf(op[12], on[13], fmaf(on[12], op[13], 1e-6f * op[13] * on[13])) * ge);
            auto r = __builtin_amdgcn_permlane32_swap(u_(on[13] * rd_), u_(op[13] * rd_), false, false);
            auto r2 = __builtin_amdgcn_permlane32_swap(r[1], r[1], false, false);
            publish_o(op * f_(r2[0]) + on * f_(r2[1]), WS_OB);
        }
        STAMP(8);
        if (p2) {
            load_o(WS_OA, 0);
            LBAR();
            wfrags(wfb, L_WOB);
            f32x4 cc[2][4] = {};
            if (PRIO) __builtin_amdgcn_s_setprio(0);
            STAMP(9);
            passes(cc, wfa, WS_OA);
            STAMP(10);
            load_o(WS_OB, 0);
            passes(cc, wfb, WS_OB);
            finish(cc);
        } else if (b == 31 && w == 0) {
#pragma nounroll
            for (int it = 0; it < SPIN; ++it) {
                const u32x4 tg = {tag.x, tag.y, tag.x, tag.y};
                const u32x4 v = (gld(rs, WS_FL + l * 16) ^ tg) | (gld(rs, WS_FL + 1024 + l * 16) ^ tg);
                if (none((v.x | v.y | v.z | v.w) != 0)) break;
                nap();
            }
            gst64(rs, l == 0 ? WS_E0 : OOB, tag);
        }
        leave();
    }

}

__global__ __launch_bounds__(512, 2) void fused(const Args ar) {
    __shared__ __attribute__((aligned(1024))) char smem[163840];
    const u32 lb = (u32)(size_t)(LDSP(char))smem;
    if (blockIdx.x >= 128) body<true>(ar, lb); else body<false>(ar, lb);
}

extern "C" void kernel_launch(void* const* d_in, const int* in_sizes, int n_in, void* d_out, int out_size, void* d_ws, size_t ws_size, hipStream_t stream) {
    (void)in_sizes; (void)n_in; (void)out_size; (void)ws_size;
    Args a;
    a.x = d_in[0]; a.wa = d_in[1]; a.wb = d_in[4]; a.wphi = d_in[5]; a.gate = d_in[7]; a.ws = d_ws; a.woa = d_in[2]; a.wob = d_in[6]; a.out = d_out; a.pad = nullptr;
    hipLaunchKernelGGL(fused, dim3(256), dim3(512), 0, stream, a);
}
```

```cpp
#include <hip/hip_runtime.h>
#include <stdint.h>

typedef unsigned u32;
typedef unsigned long long u64;
typedef u32 u32x4 __attribute__((ext_vector_type(4)));
typedef u32 u32x2 __attribute__((ext_vector_type(2)));
typedef float f32x2 __attribute__((ext_vector_type(2)));
typedef float f32x4 __attribute__((ext_vector_type(4)));
typedef float f32x16 __attribute__((ext_vector_type(16)));
typedef __bf16 bf16x8 __attribute__((ext_vector_type(8)));
typedef __bf16 bf16x2 __attribute__((ext_vector_type(2)));
typedef short s16x4 __attribute__((ext_vector_type(4)));
typedef __amdgpu_buffer_rsrc_t rsrc_t;
#define LDSP(T) __attribute__((address_space(3))) T*
#define DI __device__ __forceinline__

constexpr u32 WS_E0 = 0;
constexpr u32 WS_FL = 1024;
constexpr u32 WS_KVR = 16384;
constexpr u32 WS_KX = 256 * 1024;
constexpr u32 WS_P = 1024 * 1024;
constexpr u32 WS_OA = 3 * 1024 * 1024;
constexpr u32 WS_OB = 5 * 1024 * 1024;
constexpr u32 WS_BYTES = 7 * 1024 * 1024;
constexpr u32 slot(bool A, int c) { return A ? (c & 3) * 32768 : 16384 + ((6 - c) % 6) * 24576; }
constexpr u32 sbase(bool A, int c) { return A ? (c & 2) * 32768 : c == 1 || c == 2 ? slot(false, 2) : c == 3 || c == 4 ? slot(false, 4) : 0; }
constexpr u32 wofs(int ks) { return (6 - (ks >> 2)) * 8192 + (ks & 3) * 2048; }
constexpr u32 L_WV = 104 * 1024, L_MISC = 0, L_PHI = 0, L_VP = 32 * 1024, L_AX = 0;
constexpr u32 OOB = 0x80000000u, ONES = 0x3F803F80u;
constexpr int SPIN = 1 << 15;
constexpr int SC1 = 16;
#define AD2 0
#define W1X 1
#define VB0 2
#define PRIO 1
#define PPOLL 9
#define KPOLL 20
template <int N> struct ic { static constexpr int value = N; };
constexpr float LOG2E = 1.4426950408889634f;

DI void dma(u32 m0v, u32 voff, rsrc_t rs, u32 soff) {
    asm volatile("s_mov_b32 m0, %0\n\ts_nop 0\n\tbuffer_load_dwordx4 %1, %2, %3 offen lds" ::"s"(m0v), "v"(voff), "s"(rs), "s"(soff) : "memory");
}
DI void dmax(u32 m0v, u32 voff, rsrc_t rs, u32 soff, u64 mask) {
    asm volatile("s_mov_b32 m0, %0\n\ts_mov_b64 exec, %4\n\tbuffer_load_dwordx4 %1, %2, %3 offen lds\n\ts_mov_b64 exec, -1" ::"s"(m0v), "v"(voff), "s"(rs), "s"(soff), "s"(mask) : "memory");
}
DI u32 touch(u32 voff, rsrc_t rs) {
    u32 r; asm volatile("buffer_load_dword %0, %1, %2, 0 offen" : "=v"(r) : "v"(voff), "s"(rs) : "memory"); return r;
}
DI void drop(u32 r) { asm volatile("" ::"v"(r)); }
template <int N> DI void waitbar() { asm volatile("s_waitcnt vmcnt(%0) lgkmcnt(0)\n\ts_barrier" ::"i"(N) : "memory"); }
#define WAITBAR(N) asm volatile("s_waitcnt vmcnt(" #N ") lgkmcnt(0)\n\ts_barrier" ::: "memory")
#define VBAR(N) asm volatile("s_waitcnt vmcnt(" #N ")\n\ts_barrier" ::: "memory")
#define VM0() __builtin_amdgcn_s_waitcnt(0x0F70)
#define LBAR() asm volatile("s_waitcnt lgkmcnt(0)\n\ts_barrier" ::: "memory")
#define BAR() asm volatile("s_barrier" ::: "memory")
#define SCHED() __builtin_amdgcn_sched_barrier(0)
DI u32x4 r128(u32 a) { return *(LDSP(const u32x4))(size_t)a; }
DI void w128(u32 a, u32x4 v) { *(LDSP(u32x4))(size_t)a = v; }
DI void w64(u32 a, u32x2 v) { *(LDSP(u32x2))(size_t)a = v; }
DI u32x2 rtr(u32 a) { return __builtin_bit_cast(u32x2, __builtin_amdgcn_ds_read_tr16_b64_v4i16((LDSP(s16x4))(size_t)a)); }
DI u32x4 rtr2(u32 a, u32 b) { u32x2 x = rtr(a), y = rtr(b); return u32x4{x.x, x.y, y.x, y.y}; }
DI f32x16 mm32(u32x4 a, u32x4 b, f32x16 c) { return __builtin_amdgcn_mfma_f32_32x32x16_bf16(__builtin_bit_cast(bf16x8, a), __builtin_bit_cast(bf16x8, b), c, 0, 0, 0); }
DI f32x4 mm16(u32x4 a, u32x4 b, f32x4 c) { return __builtin_amdgcn_mfma_f32_16x16x32_bf16(__builtin_bit_cast(bf16x8, a), __builtin_bit_cast(bf16x8, b), c, 0, 0, 0); }
DI u32 pk(float lo, float hi) { f32x2 v = {lo, hi}; return __builtin_bit_cast(u32, __builtin_convertvector(v, bf16x2)); }
DI u32x4 pk8(const f32x16& a, int s) { return u32x4{pk(a[8 * s], a[8 * s + 1]), pk(a[8 * s + 2], a[8 * s + 3]), pk(a[8 * s + 4], a[8 * s + 5]), pk(a[8 * s + 6], a[8 * s + 7])}; }
DI float f_(u32 x) { return __builtin_bit_cast(float, x); }
DI u32 u_(float x) { return __builtin_bit_cast(u32, x); }
DI float max2(float a, float b) { return __builtin_elementwise_maximum(a, b); }
DI float max3(float a, float b, float c) { return max2(max2(a, b), c); }
DI float mul1(float a, float b) { float r; asm("v_mul_f32 %0, %1, %2" : "=v"(r) : "v"(a), "v"(b)); return r; }
DI u32 bfi(u32 m, u32 a, u32 b) { return (a & m) | (b & ~m); }
DI void pin(f32x16& x) { asm volatile("" : "+v"(x)); }
DI float xmax32(float x) { auto r = __builtin_amdgcn_permlane32_swap(u_(x), u_(x), false, false); return max2(f_(r[0]), f_(r[1])); }
DI float xadd32(float x) { auto r = __builtin_amdgcn_permlane32_swap(u_(x), u_(x), false, false); return f_(r[0]) + f_(r[1]); }
DI u32x4 gld(rsrc_t rs, u32 off) { return __builtin_amdgcn_raw_buffer_load_b128(rs, off, 0, SC1); }
DI void gst(rsrc_t rs, u32 off, u32x4 v) { __builtin_amdgcn_raw_buffer_store_b128(v, rs, off, 0, SC1); }
DI void gst64(rsrc_t rs, u32 off, u32x2 v) { __builtin_amdgcn_raw_buffer_store_b64(v, rs, off, 0, SC1); }
DI void gst32(rsrc_t rs, u32 off, u32 v) { __builtin_amdgcn_raw_buffer_store_b32(v, rs, off, 0, SC1); }
DI u32 opq(u32 x) { asm("" : "+v"(x)); return x; }
DI float sel(u32 m, float a, float b) { return f_((u_(a) & m) | (u_(b) & ~m)); }
DI void nap() { asm volatile("s_sleep 1" ::: "memory"); }
DI bool none(bool bad) { return __builtin_amdgcn_ballot_w64(bad) == 0; }
DI u32x4 max(u32x4 a, u32x4 b) { return __builtin_elementwise_max(a, b); }
DI bool unset(u32x4 v) { return max(max(v.x, v.y), max(v.z, v.w)) == ~0u; }
DI rsrc_t mk(const void* p, u32 bytes) { return __builtin_amdgcn_make_buffer_rsrc((void*)p, 0, bytes, 0x00020000); }

template <bool NORM, class H, class E>
DI void phi(const f32x16& p, float keep, H hook, E emit) {
    float mx = max2(p[0], p[1]), nm = max2(-p[0], -p[1]);
    auto mstep = [&](int i) { mx = max3(p[i], p[i + 1], mx); nm = max3(-p[i], -p[i + 1], nm); };
    mstep(2); hook(ic<0>{}); mstep(4); mstep(6); hook(ic<1>{}); mstep(8); mstep(10); hook(ic<2>{}); mstep(12); mstep(14); hook(ic<3>{});
    {
        auto r = __builtin_amdgcn_permlane32_swap(u_(mx), u_(nm), false, false);
        const float m = max2(f_(r[0]), f_(r[1])) * -LOG2E;
        auto q = __builtin_amdgcn_permlane32_swap(u_(m), u_(m), false, false);
        mx = f_(q[0]); nm = f_(q[1]);
    }
    hook(ic<4>{});
    float a[16], b[16];
    float sa = 0.f, sb = 0.f;
    auto step = [&](auto ii) {
        constexpr int i = 15 - decltype(ii)::value;
        a[i] = __builtin_amdgcn_exp2f(fmaf(p[i], LOG2E, mx)); b[i] = __builtin_amdgcn_exp2f(fmaf(p[i], -LOG2E, nm));
        if (NORM) {
            if constexpr (i == 13) { sa = a[15] + a[14]; sb = b[15] + b[14]; }
            else if constexpr (i == 10) { sa = fmaf(sa, keep, a[11]); sb = fmaf(sb, keep, b[11]); }
            else if constexpr (i < 13) { sa += a[i + 1]; sb += b[i + 1]; }
        }
        hook(ic<20 - i>{});
    };
    step(ic<0>{}); step(ic<1>{}); step(ic<2>{}); step(ic<3>{}); step(ic<4>{}); step(ic<5>{}); step(ic<6>{}); step(ic<7>{});
    step(ic<8>{}); step(ic<9>{}); step(ic<10>{}); step(ic<11>{}); step(ic<12>{}); step(ic<13>{}); step(ic<14>{}); step(ic<15>{});
    if (NORM) {
        sa += a[0]; sb += b[0];
        auto r = __builtin_amdgcn_permlane32_swap(u_(sa), u_(sb), false, false);
        const float m = __builtin_amdgcn_rcpf(f_(r[0]) + f_(r[1]));
        auto q = __builtin_amdgcn_permlane32_swap(u_(m), u_(m), false, false);
        sa = f_(q[0]); sb = f_(q[1]);
    }
    auto out = [&](auto ss) {
        constexpr int s = decltype(ss)::value;
        hook(ic<21 + s>{});
        float* e = (s & 2) ? b + 8 * (s & 1) : a + 8 * (s & 1);
        if (NORM) {
#pragma unroll
            for (int i = 0; i < 8; ++i) e[i] = mul1(e[i], (s & 2) ? sb : sa);
        }
        emit(ss, u32x4{pk(e[0], e[1]), pk(e[2], e[3]), pk(e[4], e[5]), pk(e[6], e[7])});
    };
    out(ic<0>{}); out(ic<1>{}); out(ic<2>{}); out(ic<3>{});
}

#define STAMP(i)
#define STAMPB(i)

struct Args { const void *x, *wa, *wb, *wphi, *gate; void* ws; const void *woa, *wob; void* out; const void* pad; };
template <bool isA>
DI void body(const Args& ar, const u32 lb) {
    STAMP(0);
    const u32 tst0 = (u32)__builtin_amdgcn_s_memrealtime();
    const u32 tid = threadIdx.x, l = tid & 63;
    const u32 w = __builtin_amdgcn_readfirstlane(tid >> 6);
    const u32 b = blockIdx.x, xcd = b & 7, bi = b >> 3;
    const void *gx = ar.x, *gwa = ar.wa, *gwb = ar.wb, *gwphi = ar.wphi, *ggate = ar.gate, *gwoa = ar.woa, *gwob = ar.wob;
    void *gws = ar.ws, *gout = ar.out;
    asm volatile("" : "+s"(gx), "+s"(gwa), "+s"(gwb), "+s"(gwphi), "+s"(ggate), "+s"(gws), "+s"(gwoa), "+s"(gwob), "+s"(gout));
    const u32 t = (xcd >> 1) * 4 + ((bi & 15) >> 2), j = (xcd & 1) * 4 + (bi & 3);
    const u32 rg = w & 3, hh = w >> 2, head = 2 * j + hh;
    const u32 tok = l & 31, h = l >> 5, p = l & 15, g = l >> 4;
    const rsrc_t rx = mk(gx, 2048 * 896), rw = mk(isA ? gwa : gwb, 448 * 2688), rs = mk(gws, WS_BYTES);
    constexpr u32 L_WQ = isA ? 104 * 1024 : 48 * 1024, L_WOA = isA ? 48 * 1024 : 104 * 1024, L_WOB = isA ? 104 * 1024 : 0, L_XO = isA ? 0 : 56 * 1024;
    const u32 a16 = bi & 15, jb = (xcd & 1) * 3;
    const u32 un = t >> 1, cg = (t & 1) * 8 + w, gq = cg >> 2;
    constexpr u32 KV2 = 512;
    const u32 kvd = 16 * (un & 1) + ((4 * cg + (l & 3)) & 15);
    const u32 kvo = WS_KVR + ((2 * j + (un >> 2)) * 4 + ((un >> 1) & 1) * 2 + (gq >> 1)) * 1024 + (kvd + 32 * (gq & 1)) * 16 + g * 4;
    u32 jk0 = 0, jk1 = 0, jk2 = 0;
    if constexpr (isA) {
        const rsrc_t rwb = mk(gwb, 448 * 2688);
        const bool sp = w == 0 && a16 < 3;
        const u32 o1 = sp ? (a16 < 2 ? (xcd & 1) * 12544 + a16 * 8192 : 0) + l * 128
                     : a16 < 8 ? ((t & 12) * 128 + a16 * 64 + l) * 896 + w * 128
                               : (w * 64 + (a16 & 7) * 8 + (l >> 3)) * 2688 + (((l >> 2) & 1) * 7 + 7 + jb + (l & 3)) * 128;
        jk0 = touch(sp || (w < 7 && (w >= W1X || (a16 >= 8 && (l & 4)))) ? o1 : OOB, sp ? (a16 < 2 ? mk(gwphi, 16 * 1568) : mk(ggate, 896)) : a16 < 8 ? rx : rwb);
        const u64 clk0 = __builtin_amdgcn_s_memtime();
        BAR();
        jk1 = touch(l < 32 ? (w < 7 ? (w * 64 + a16 * 4 + (l >> 3)) * 2688 + (((l >> 2) & 1) * 7 + 7 + jb + (l & 3)) * 128 : OOB) : OOB, rw);
        BAR();
        jk2 = touch(w < 2 && (w * 16 + (l >> 2)) < 28 ? (a16 * 28 + w * 16 + (l >> 2)) * 2688 + (jb + (l & 3)) * 128 : OOB, rwb);
#pragma nounroll
        for (int it = 0; it < 256 && __builtin_amdgcn_s_memtime() - clk0 < AD2; ++it) nap();
        BAR();
    } else gst32(rs, (l & 8) || w >= 4 ? OOB : kvo + (l & 4) * (KV2 / 4), ~0u);

    const u32 vx0 = (t * 128 + w * 16 + (l >> 3)) * 896 + (((l & 7) ^ g) << 4);
    const u32 vx1 = (vx0 + 8 * 896) ^ 64;
    const u32 wsrc = (l & 7) ^ ((g & 1) << 2);
    const u32 vw = wsrc == 7 ? OOB : (w * 8 + (l >> 3)) * 2688 + j * 112 + wsrc * 16;
    auto chunk1 = [&](int c, int q) {
        const u32 sl = lb + slot(isA, c);
        if (q == 0) dma(sl + w * 2048, vx0, rx, c * 128);
        if (q == 1) dma(sl + w * 2048 + 1024, vx1, rx, c * 128);
        if (q == 2) dma(sl + 16384 + w * 1024, vw, rw, c * (64 * 2688) + 896);
        if (q == 3 && isA) dma(sl + 24576 + w * 1024, vw, rw, c * (64 * 2688) + 1792);
    };
    auto chunk = [&](int c) { chunk1(c, 0); chunk1(c, 1); chunk1(c, 2); chunk1(c, 3); };
    auto wv_dma = [&](int q) { dma(lb + L_WV + wofs(4 * q) + w * 1024, vw, rw, q * (64 * 2688) + 1792); };
    auto wq_dma = [&](int q) { dmax(lb + L_WQ + wofs(4 * q) + w * 1024, vw, rw, q * (64 * 2688), 0xF7F77F7FF7F77F7Full); };
    chunk(0);
    BAR();
    STAMP(1);
    chunk(1);
    BAR();
    STAMP(14);
    chunk(2);
    STAMP(13);
    u64 tg = *(const __attribute__((address_space(4))) u64*)(size_t)gws + 0x9E3779B97F4A7C15ull;
    asm volatile("" : "+s"(tg));
    const u32x2 tag = {(u32)tg, (u32)(tg >> 32)};
    u32 gatev = 0;
    auto misc = [&]() {
        if (!isA && !hh) dma(lb + L_MISC + w * 1024, (w & 1) ? (l < 34 ? l * 16 + 1024 : OOB) : l * 16, mk(gwphi, 16 * 1568), (2 * j + (w >> 1)) * 1568);
        else gatev = touch(tid * 4 - 1024, mk(ggate, 896));
    };
    auto gate_col = [&]() {
        asm volatile("" : "+v"(gatev));
        if (tid - 256 < 224) {
            const u32 r = 2 * tid - 512, ga = lb + L_WQ + (6 - (r >> 6)) * 8192 + (r & 63) * 128 + ((7 ^ ((r & 2) << 1)) << 4);
            w128(ga, u32x4{gatev & 0xFFFFu, 0, 0, 0}); w128(ga + 128, u32x4{gatev >> 16, 0, 0, 0});
        }
    };
    auto flag = [&]() { gst64(rs, tid == 0 ? WS_FL + b * 8 : OOB, tag); };

    const u32 xa = lb + (rg * 32 + tok) * 128 + ((h ^ ((tok >> 1) & 7)) << 4);
    const u32 wrow = lb + (8 * h + (p >> 2)) * 128, wcol = 32 * (g & 1) + 8 * (p & 3), wsw = ((p >> 3) & 1) << 6;
    const u32 wa = wrow + ((56 * hh + wcol) ^ wsw);
    u32x4 xf[28], wphi[2];
    f32x16 ak = {}, av = {};
    u32x4 wk[2][4], wv[4];
    u32 xb, wb;
    auto rd = [&](int c, int s) {
        const u32 sl = slot(isA, c) - sbase(isA, c);
        xf[4 * c + s] = r128((xb ^ (32 * s)) + sl);
        wk[isA ? 0 : c & 1][s] = rtr2(wb + sl + 16384 + s * 2048, wb + sl + 16384 + s * 2048 + 512);
        if (isA) wv[s] = rtr2(wb + sl + 24576 + s * 2048, wb + sl + 24576 + s * 2048 + 512);
    };
    auto mk_ = [&](int c, int s) { if (c >= 0) { ak = mm32(wk[isA ? 0 : c & 1][s], xf[4 * c + s], ak); pin(ak); } SCHED(); };
    auto mv_ = [&](int ks) { if (ks >= 0) { av = mm32(xf[ks], wv[ks & 3], av); pin(av); } SCHED(); };
    auto issue = [&](int c, int q) {
        if (c < 4) { chunk1(c + 3, q); if (!isA && c == 3 && q == 3) misc(); }
        else if (isA) {
            const int n = 4 * (c - 4) + q;
            if (n == 0) misc(); else if (n < 8) wq_dma(n - 1);
            else if (n == 8) jk0 = touch(l < 25 ? (a16 * 196 + w * 25 + l) * 128 : OOB, mk(gwob, 448 * 896));
        } else if (q < 3) {
            const int n = 3 * (c - 4) + q;
            if (n < 7) wv_dma(n);
            else if (n == 7) jk0 = touch(l < 16 && w < 7 ? (a16 * 28 + w * 4 + (l >> 2)) * 2688 + (jb + (l & 3)) * 128 : OOB, mk(gwa, 448 * 2688));
            else jk1 = touch(l < 25 ? (a16 * 196 + w * 25 + l) * 128 : OOB, mk(gwoa, 448 * 896));
        }
        SCHED();
    };
    auto iter = [&](auto cc) {
        constexpr int c = decltype(cc)::value;
        constexpr int W[7] = {6, 6, 6, 6, 7, 7, 6};
        if (c == 0 || sbase(isA, c) != sbase(isA, c - 1)) { xb = opq(xa + sbase(isA, c)); wb = opq(wa + sbase(isA, c)); }
        waitbar<isA ? 8 : W[c]>();
        if (c == 0) { drop(jk0); drop(jk1); drop(jk2); }
        STAMP(16 + c);
        rd(c, 0); rd(c, 1);
        SCHED();
        if constexpr (isA) {
            mk_(c - 1, 2); issue(c, 0); mv_(4 * c - 2); issue(c, 1);
            mk_(c - 1, 3); issue(c, 2); mv_(4 * c - 1); issue(c, 3);
            rd(c, 2); rd(c, 3);
            SCHED();
            mk_(c, 0); mv_(4 * c); mk_(c, 1); mv_(4 * c + 1);
        } else {
            mk_(c - 1, 1);
            issue(c, 0);
            rd(c, 2); rd(c, 3);
            SCHED();
            mk_(c - 1, 2);
            issue(c, 1);
            mk_(c - 1, 3);
            issue(c, 2);
            if (c == 6) {
                const u32 fq = 4 * (g & 1) + (p & 3);
                const u32 pa = lb + L_MISC + hh * 2048 + (4 * h + (p >> 2)) * 56 + (fq == 7 ? 6 : fq) * 8;
                wphi[0] = rtr2(pa, pa + 8 * 56); wphi[1] = rtr2(pa + 16 * 56, pa + 24 * 56);
                gate_col();
            }
            SCHED();
            mk_(c, 0);
            issue(c, 3);
        }
    };
    iter(ic<0>{}); iter(ic<1>{}); iter(ic<2>{}); iter(ic<3>{}); iter(ic<4>{}); iter(ic<5>{}); iter(ic<6>{});
    if (!isA) mk_(6, 1);
    mk_(6, 2); mk_(6, 3);
    if (isA) { mv_(26); mv_(27); }
    const u32x4 kf0 = pk8(ak, 0), kf1 = pk8(ak, 1);
    STAMP(2);

    const u32 i2 = (bi + 28) & 31;
    const bool p2 = i2 < 28;
    const u32 u2 = xcd * 4 + i2 / 7, cb = i2 % 7;
    const u32 vwo = (rg * 8 + (l >> 3)) * 896 + cb * 128 + (((l & 7) ^ (g << 1)) << 4);
    const rsrc_t roa = mk(gwoa, p2 ? 448 * 896 : 0), rob = mk(gwob, p2 ? 448 * 896 : 0);
    auto wob_dma = [&]() {
#pragma unroll
        for (int n = 0; n < 7; ++n) dma(lb + L_WOB + n * 8192 + w * 1024, vwo, rob, (2 * n + hh) * (32 * 896));
    };
    auto stage2 = [&](auto early) {
        f32x16 aq = {};
        const u32 wq = opq(wrow + L_WQ + ((wcol == 56 ? 112 : 56 * hh + wcol) ^ wsw));
        u32x4 f[3];
        auto rq = [&](int ks) { if (ks < 28) f[ks % 3] = rtr2(wq + wofs(ks), wq + wofs(ks) + 512); };
        if (PRIO && !hh) __builtin_amdgcn_s_setprio(1);
        if (hh) {
#pragma unroll
            for (int n = 0; n < 14; ++n) dma(lb + L_WOA + n * 4096 + rg * 1024, vwo, roa, n * (32 * 896));
        }
        rq(0); rq(1);
#pragma unroll
        for (int ks = 0; ks < 28; ++ks) {
            rq(ks + 2);
            aq = mm32(f[ks % 3], xf[ks], aq);
            pin(aq);
            SCHED();
            if (ks == KPOLL) early();
            SCHED();
        }
        return aq;
    };
    auto tagged = [&](u32x4& a, u32x4& c) {
        auto r = __builtin_amdgcn_permlane32_swap(c.z, c.w, false, false);
        c.z = r[0]; c.w = bfi(0xFFFF0000u, a.w, tag.y); a.w = bfi(0x0000FFFFu, a.w, tag.y);
    };
    const u32 ooff = ((t * 8 + rg * 2 + (tok >> 4)) * 16 + head) * 1024 + h * 256 + p * 16;
    auto publish_o = [&](const f32x16& o, u32 base) {
        u32x4 o0 = pk8(o, 0), o1 = pk8(o, 1);
        tagged(o0, o1);
        gst(rs, base + ooff, o0); gst(rs, base + ooff + 512, o1);
    };
    const u32 th = w & 1, kq = w >> 1;
    const u32 oo = ((u2 * 4 + th * 2) * 16 + kq * 4) * 1024 + l * 16;
    u32x4 of[8];
    auto load_o = [&](u32 base, u32 from) {
#pragma unroll
        for (u32 e = 0; e < 8; ++e) of[e] = gld(mk(gws, e >= from ? WS_BYTES : 0), base + oo + (e >> 1) * 1024 + (e & 1) * 16384);
    };
    auto wfrags = [&](u32x4 (&wf)[4][4], u32 img) {
        const u32 i = p >> 2, r0 = 16 * (g >> 1) + 4 * (g & 1) + i, r1 = g < 2 ? r0 + 8 : i < 2 ? 24 + 2 * (g & 1) + i : 8 + 4 * (g & 1) + i;
        const u32 c = lb + img + 112 * 128 * kq + (p & 1) * 8;
        const u32 wb0 = c + r0 * 128 + ((((p & 3) >> 1) ^ (r0 & 6)) << 4), wb1 = c + r1 * 128 + ((((p & 3) >> 1) ^ (r1 & 6)) << 4);
        u32 wr0[4], wr1[4];
#pragma unroll
        for (int x = 0; x < 4; ++x) { wr0[x] = opq(wb0 ^ (x << 5)); wr1[x] = opq(wb1 ^ (x << 5)); }
#pragma unroll
        for (int e = 0; e < 4; ++e)
#pragma unroll
            for (int nt = 0; nt < 4; ++nt) { const int x = nt ^ (2 * (e & 1)); wf[e][nt] = rtr2(wr0[x] + e * 3584, wr1[x] + e * 3584); }
    };

    auto passes = [&](f32x4 (&cc)[2][4], const u32x4 (&wf)[4][4], u32 base) {
        const u32 tm = g < 2 ? 0xFFFF0000u : 0x0000FFFFu, ts = tag.y & tm;
        auto pass = [&](u32 from) -> u32 {
#pragma unroll
            for (u32 e = 0; e < 8; ++e) {
                if (e < from) continue;
                u32x4 f = of[e];
                f.w ^= ts;
                if (!none((f.w & tm) != 0)) return e;
#pragma unroll
                for (int nt = 0; nt < 4; ++nt) cc[e & 1][nt] = mm16(wf[e >> 1][nt], f, cc[e & 1][nt]);
            }
            return 8;
        };
        u32 e = 0;
#pragma nounroll
        for (int it = 0;; ++it) {
            e = pass(e);
            if (e == 8 || it == SPIN) break;
            asm volatile("" ::: "memory");
            load_o(base, e);
        }
    };
    auto leave = [&]() {
        if (!isA) VM0();
        if (b == 0 && tid == 0) __hip_atomic_store((u32*)((char*)gws + (17u << 20)), tst0 - 100000, __ATOMIC_RELAXED, __HIP_MEMORY_SCOPE_AGENT);
    };
    auto finish = [&](const f32x4 (&cc)[2][4]) {
        STAMP(11);
        const u32 xo = lb + L_XO + kq * 16384 + (th * 32 + p) * 256 + ((g ^ (p & 7)) << 4);
#pragma unroll
        for (int nt = 0; nt < 4; ++nt) { w128(xo ^ (nt << 6), __builtin_bit_cast(u32x4, cc[0][nt])); w128((xo ^ (nt << 6)) + 4096, __builtin_bit_cast(u32x4, cc[1][nt])); }
        LBAR();
        const u32 tk = tid >> 3, c8 = tid & 7;
        const u32 xr = lb + L_XO + tk * 256 + (((2 * c8) ^ (tk & 7)) << 4);
        auto rsum = [&](u32 a) { auto f = [&](u32 o) { return __builtin_bit_cast(f32x4, r128(a + o)); }; return (f(0) + f(16384)) + (f(32768) + f(49152)); };
        const f32x4 r0 = rsum(xr), r1 = rsum(xr ^ 16);
        gst(mk(gout, 2048 * 896), (u2 * 64 + tk) * 896 + cb * 128 + c8 * 16, u32x4{pk(r0[0], r0[1]), pk(r0[2], r0[3]), pk(r1[0], r1[1]), pk(r1[2], r1[3])});
        STAMP(12);
    };

    if constexpr (isA) {
        const u32 kxo = WS_KX + (t * 16 + head) * 3072 + l * 16;
        const u32x4 vf0 = pk8(av, 0), vf1 = pk8(av, 1);
        const u32 mlo = h ? 0u : 0x0000FFFFu, mhi = h ? 0xFFFF0000u : 0u;
        asm volatile("s_waitcnt vmcnt(1)" ::: "memory");
        gate_col();
        if (rg < 3) {
            const u32 ax = lb + L_AX + w * 3072 + l * 16;
            w128(ax, kf0); w128(ax + 1024, kf1); w128(ax + 2048, vf1);
        } else {
            u32x4 a = kf0, c = kf1;
            tagged(a, c); gst(rs, kxo, a); gst(rs, kxo + 1024, c);
            auto r = __builtin_amdgcn_permlane32_swap(vf1.w, vf1.x, false, false);
            gst(rs, kxo + 2048, u32x4{bfi(mlo, tag.y, r[1]), vf1.y, vf1.z, bfi(mhi, tag.y, vf1.w)});
        }
        if (w == 0) flag();
        LBAR();
        STAMP(3);
        u32x4 kp0, kp1, vp;
        const bool kx = rg == 0 && t > 0;
        const u32 kpo = kxo - 16 * 3072;
        const rsrc_t rk = mk(gws, kx ? WS_BYTES : 0);
        const f32x16 aq = stage2([&]() { kp0 = gld(rk, kpo); kp1 = gld(rk, kpo + 1024); vp = gld(rk, kpo + 2048); });
        STAMP(4);
        if (kx) {
#pragma nounroll
            for (int it = 0;; ++it) {
                if (none(((bfi(0xFFFF0000u, kp0.w, kp1.w) ^ tag.y) | ((bfi(mlo, vp.x, vp.w) ^ tag.y) & (mlo | mhi))) != 0) | (it == SPIN)) break;
                asm volatile("" ::: "memory");
                kp0 = gld(rs, kpo); kp1 = gld(rs, kpo + 1024); vp = gld(rs, kpo + 2048);
            }
            VM0();
            kp0.w = bfi(0x0000FFFFu, kp0.w, kp1.w);
            kp1.w = __builtin_amdgcn_permlane32_swap(kp1.z, kp1.z, false, false)[1];
            vp.x &= ~mlo;
            vp.w = bfi(mhi, __builtin_amdgcn_permlane32_swap(vp.x, vp.x, false, false)[0], vp.w);
        } else {
            const u32 ax = lb + L_AX + (rg ? w - 1 : w) * 3072 + l * 16;
            kp0 = r128(ax); kp1 = r128(ax + 1024); vp = r128(ax + 2048);
        }
        STAMP(5);
        const float ge = 1.f + __builtin_amdgcn_exp2f(aq[12] * -LOG2E);
        u32x4 qf0 = pk8(aq, 0), qf1 = pk8(aq, 1);
        if (h) { qf1.z = 0; qf1.w = 0; }
        f32x16 so = mm32(kf0, qf0, f32x16{}); so = mm32(kf1, qf1, so);
        f32x16 sp = mm32(kp0, qf0, f32x16{}); sp = mm32(kp1, qf1, sp);
        const u32 om = opq(0u - (tok & 1));
        const u32 base = (tok & ~1u) - 4 * h, basep = (rg > 0 || t > 0) ? base + 32 : 1000u;
        float sv[12];
        const float NEG = -1e30f;
#pragma unroll
        for (int i = 0; i < 8; ++i) { const float v = sel(om, so[2 * i + 1], so[2 * i]); sv[i] = (base - (8 * (i >> 1) + 2 * (i & 1))) <= 14u ? v : NEG; }
#pragma unroll
        for (int i = 4; i < 8; ++i) { const float v = sel(om, sp[2 * i + 1], sp[2 * i]); sv[4 + i] = (basep - (8 * (i >> 1) + 2 * (i & 1))) <= 14u ? v : NEG; }
        float mx = max2(sv[0], sv[1]);
#pragma unroll
        for (int i = 2; i < 12; i += 2) mx = max3(sv[i], sv[i + 1], mx);
        const float sc = 0.18898223650461362f * LOG2E;
        mx = xmax32(mx) * -sc;
        float sum = 0.f;
        u32 pd[12];
        const u32 pm = om ^ 0x0000FFFFu;
#pragma unroll
        for (int i = 0; i < 12; ++i) { const float e = __builtin_amdgcn_exp2f(fmaf(sv[i], sc, mx)); sum += e; pd[i] = pk(e, e) & pm; }
        f32x16 o = mm32(vf0, u32x4{pd[0], pd[1], pd[2], pd[3]}, f32x16{});
        o = mm32(vf1, u32x4{pd[4], pd[5], pd[6], pd[7]}, o);
        o = mm32(vp, u32x4{pd[8], pd[9], pd[10], pd[11]}, o);
        const u32 cf = u_(__builtin_amdgcn_rcpf(xadd32(sum) * ge));
        publish_o(o * f_(__builtin_amdgcn_permlane32_swap(cf, cf, false, false)[1]), WS_OA);
        STAMP(8);
        VBAR(2);
        drop(jk0);
        wob_dma();
        load_o(WS_OA, 0);
        u32x4 wf[4][4];
        f32x4 cc[2][4] = {};
        wfrags(wf, L_WOA);
        if (PRIO) __builtin_amdgcn_s_setprio(0);
        STAMP(9);
        passes(cc, wf, WS_OA);
        STAMP(10);
        WAITBAR(0);
        wfrags(wf, L_WOB);
#pragma nounroll
        for (int it = 0; it < SPIN; ++it) {
            const u32 v = __builtin_amdgcn_raw_buffer_load_b32(rs, l < 4 ? WS_KVR + (kq * 4 + l) * 4096 : OOB, 0, SC1);
            if (none(v == ~0u)) break;
            nap();
        }
        load_o(WS_OB, 0);
        passes(cc, wf, WS_OB);
        finish(cc);
        leave();
    } else {
        const float keep = h ? 0.f : 1.f;
        {
            f32x16 pp = mm32(wphi[0], kf0, f32x16{});
            pp = mm32(wphi[1], kf1, pp);
            pin(pp);
            STAMP(23);
            const u32 wvb = opq(wa + L_WV);
            auto rdv = [&](int ks) { if (ks < 28) wv[ks & 3] = rtr2(wvb + wofs(ks), wvb + wofs(ks) + 512); };
            const u32 F = (((tok >> 1) & 3) << 2) | (((tok >> 3) & 1) << 1) | (tok & 1);
            const u32 pw = lb + L_PHI + w * 4096 + tok * 128 + ((h ^ F) << 3);
            SCHED();
            phi<true>(pp, keep, [&](auto nn) {
                constexpr int n = decltype(nn)::value;
                constexpr int k0 = n <= VB0 ? 0 : (n - VB0 - 1) * 28 / (24 - VB0), k1 = n <= VB0 ? 0 : (n - VB0) * 28 / (24 - VB0);
                SCHED();
                if (n == VB0) { STAMPB(1); VBAR(6); STAMPB(14); rdv(0); rdv(1); }
#pragma unroll
                for (int k = k0; k < k1; ++k) {
                    if (k == 10) { STAMPB(13); VBAR(2); STAMPB(16); }
                    if (k == 20) STAMPB(17);
                    rdv(k + 2); mv_(k);
                    if (k >= 10 && k < 24 && !(k & 1)) wq_dma((k - 10) / 2);
                }
                SCHED();
            }, [&](auto ss, u32x4 v) {
                constexpr u32 s = decltype(ss)::value, cu = 8 * (s >> 1) + 2 * (s & 1);
                w64(pw ^ (cu << 3), u32x2{v.x, v.y});
                w64(pw ^ ((cu + 4) << 3), u32x2{v.z, v.w});
            });
            STAMPB(18);
            u32x4 vf0 = pk8(av, 0), vf1 = pk8(av, 1);
            if (tok == 28) { vf0 = u32x4{ONES, ONES, ONES, ONES}; vf1 = vf0; }
            w128(lb + L_VP + w * 2048 + l * 16, vf0); w128(lb + L_VP + w * 2048 + 1024 + l * 16, vf1);
        }
        STAMPB(19);
        LBAR();
        STAMP(3);
        {
            const u32 sign = rg >> 1, dt = rg & 1;
            const u32 F = ((2 * (g & 1) + (p >> 3)) << 2) | ((p >> 2) & 1);
            const u32 pr = lb + L_PHI + hh * 16384 + (16 * (g >> 1) + 4 * (g & 1) + (p >> 2)) * 128 + (((8 * sign + (p & 3)) ^ F) << 3);
            const u32 vr = lb + L_VP + hh * 8192 + g * 512 + (16 * dt + p) * 16;
            f32x4 c0 = {}, c1 = {};
#pragma unroll
            for (int r = 0; r < 4; ++r) {
                const u32x4 vv = r128(vr + r * 2048);
                c0 = mm16(rtr2(pr + r * 4096, (pr ^ 16) + r * 4096 + 1024), vv, c0);
                c1 = mm16(rtr2((pr ^ 32) + r * 4096, (pr ^ 48) + r * 4096 + 1024), vv, c1);
            }
            const u32 po = WS_P + (j * 8 + w) * 32768 + t * 1024 + l * 16;
#define TG(x, tt) bfi(0xFFFFF000u, u_(x), tt)
            gst(rs, po, u32x4{TG(c0[0], tag.x), TG(c0[1], tag.x >> 12), TG(c0[2], tag.y), TG(c0[3], tag.y >> 12)});
            gst(rs, po + 16384, u32x4{TG(c1[0], tag.x), TG(c1[1], tag.x >> 12), TG(c1[2], tag.y), TG(c1[3], tag.y >> 12)});
        }
        flag();
        STAMPB(20);
        WAITBAR(3);
        drop(jk0); drop(jk1);
        STAMP(4);
        const f32x16 aq = stage2([&]() {});
        STAMP(5);
        const u32 po = WS_P + (j * 8 + un) * 32768 + (l >> 2) * 1024 + (4 * cg + (l & 3)) * 16;
        const rsrc_t rsr = mk(gws, hh ? 0 : WS_BYTES);
        u32x4 pa[4];
        u32 tgm = hh ? 0u : 0xFFFu;
        asm("" : "+s"(tgm));
        auto ldp = [&]() { pa[0] = gld(rsr, po); pa[1] = gld(rsr, po + 16384); pa[2] = gld(rsr, po + 256); pa[3] = gld(rsr, po + 16384 + 256); };
        u32x4 pq[4];
        float ge = 1.f + __builtin_amdgcn_exp2f(aq[12] * LOG2E);
        {
            f32x16 pp = mm32(wphi[0], pk8(aq, 0), f32x16{});
            pp = mm32(wphi[1], pk8(aq, 1), pp);
            phi<false>(pp, keep, [&](auto nn) { if (decltype(nn)::value == PPOLL) { SCHED(); ldp(); SCHED(); } }, [&](auto ss, u32x4 v) { pq[decltype(ss)::value] = v; });
            asm volatile("" : "+v"(pq[0]), "+v"(pq[1]), "+v"(pq[2]), "+v"(pq[3]), "+v"(ge));
        }
        STAMP(15);
#pragma nounroll
        for (int it = 0;; ++it) {
            const u32x4 d = (pa[0] ^ pa[1]) | (pa[0] ^ pa[2]) | (pa[0] ^ pa[3]) | (pa[0] ^ u32x4{tag.x, tag.x >> 12, tag.y, tag.y >> 12});
            if (none(((d.x | d.y | d.z | d.w) & tgm) != 0) | (it == SPIN)) break;
            asm volatile("" ::: "memory");
            ldp();
        }
        VM0();
        STAMP(6);
        if (!hh) {
#pragma unroll
            for (int z = 0; z < 2; ++z) {
                const bool pad = z && (t & 1);
                const u32 mb = pad ? 0u : 0xFFFFF000u, cbit = pad ? 0u : 0x800u;
                float uu[4];
#pragma unroll
                for (int i = 0; i < 4; ++i) {
                    auto r = __builtin_amdgcn_permlane32_swap((pa[2 * z][i] & 0xFFFFF000u) | 0x800u, (pa[2 * z + 1][i] & mb) | cbit, false, false);
                    uu[i] = f_(r[0]) + f_(r[1]);
                }
                float ww[2];
#pragma unroll
                for (int i = 0; i < 2; ++i) {
                    auto r = __builtin_amdgcn_permlane16_swap(u_(uu[i]), u_(uu[i + 2]), false, false);
                    float y = f_(r[0]) + f_(r[1]);
                    y += f_(__builtin_amdgcn_update_dpp(0, u_(y), 0x128, 0xf, 0xf, false));
                    y += f_(__builtin_amdgcn_update_dpp(0, u_(y), 0x124, 0xf, 0xf, false));
                    ww[i] = y;
                }
                gst32(rs, (l & 12) ? OOB : kvo + z * KV2, kvd == 29 ? (pad && h ? 0u : ONES) : pk(ww[0], ww[1]));
            }
        }
        BAR();
        wob_dma();
        u32x4 wfa[4][4], wfb[4][4];
        {
            const u32 ko = WS_KVR + head * 4096 + l * 16;
            u32x4 kv[4];
            auto ldk = [&]() { kv[0] = gld(rs, ko); kv[1] = gld(rs, ko + 1024); kv[2] = gld(rs, ko + 2048); kv[3] = gld(rs, ko + 3072); };
            ldk();
            wfrags(wfa, L_WOA);
#pragma nounroll
            for (int it = 0;; ++it) {
                if (none(unset(max(max(kv[0], kv[1]), max(kv[2], kv[3])))) | (it == SPIN)) break;
                asm volatile("" ::: "memory");
                ldk();
            }
            STAMP(7);
            f32x16 op = mm32(kv[0], pq[0], f32x16{}), on = mm32(kv[2], pq[2], f32x16{});
            op = mm32(kv[1], pq[1], op); on = mm32(kv[3], pq[3], on);
            const float rd_ = __builtin_amdgcn_rcpf(fmaf(op[12], on[13], fmaf(on[12], op[13], 1e-6f * op[13] * on[13])) * ge);
            auto r = __builtin_amdgcn_permlane32_swap(u_(on[13] * rd_), u_(op[13] * rd_), false, false);
            auto r2 = __builtin_amdgcn_permlane32_swap(r[1], r[1], false, false);
            publish_o(op * f_(r2[0]) + on * f_(r2[1]), WS_OB);
        }
        STAMP(8);
        if (p2) {
            load_o(WS_OA, 0);
            LBAR();
            wfrags(wfb, L_WOB);
            f32x4 cc[2][4] = {};
            if (PRIO) __builtin_amdgcn_s_setprio(0);
            STAMP(9);
            passes(cc, wfa, WS_OA);
            STAMP(10);
            load_o(WS_OB, 0);
            passes(cc, wfb, WS_OB);
            finish(cc);
        } else if (b == 31 && w == 0) {
#pragma nounroll
            for (int it = 0; it < SPIN; ++it) {
                const u32x4 tg = {tag.x, tag.y, tag.x, tag.y};
                const u32x4 v = (gld(rs, WS_FL + l * 16) ^ tg) | (gld(rs, WS_FL + 1024 + l * 16) ^ tg);
                if (none((v.x | v.y | v.z | v.w) != 0)) break;
                nap();
            }
            gst64(rs, l == 0 ? WS_E0 : OOB, tag);
        }
        leave();
    }

}

__global__ __launch_bounds__(512, 2) void fused(const Args ar) {
    __shared__ __attribute__((aligned(1024))) char smem[163840];
    const u32 lb = (u32)(size_t)(LDSP(char))smem;
    if (blockIdx.x >= 128) body<true>(ar, lb); else body<false>(ar, lb);
}

__global__ void k1(u32* p) { const u32 t = (u32)__builtin_amdgcn_s_memrealtime(); if (threadIdx.x == 0) __hip_atomic_store(p, t, __ATOMIC_RELAXED, __HIP_MEMORY_SCOPE_AGENT); }
__global__ void k3(u32* p) { const u32 t0 = __hip_atomic_load(p, __ATOMIC_RELAXED, __HIP_MEMORY_SCOPE_AGENT); for (int i = 0; i < 100000 && (u32)__builtin_amdgcn_s_memrealtime() - t0 < 4000; ++i) nap(); }
extern "C" void kernel_launch(void* const* d_in, const int* in_sizes, int n_in, void* d_out, int out_size, void* d_ws, size_t ws_size, hipStream_t stream) {
    (void)in_sizes; (void)n_in; (void)out_size; (void)ws_size;
    Args a;
    a.x = d_in[0]; a.wa = d_in[1]; a.wb = d_in[4]; a.wphi = d_in[5]; a.gate = d_in[7]; a.ws = d_ws; a.woa = d_in[2]; a.wob = d_in[6]; a.out = d_out; a.pad = nullptr;
    hipLaunchKernelGGL(fused, dim3(256), dim3(512), 0, stream, a);
    hipLaunchKernelGGL(k3, dim3(1), dim3(64), 0, stream, (u32*)((char*)d_ws + (17u << 20)));
}
```

```cpp
#define CPF 0
#define KO 1
#include <hip/hip_runtime.h>
#include <stdint.h>

typedef unsigned u32;
typedef unsigned long long u64;
typedef u32 u32x4 __attribute__((ext_vector_type(4)));
typedef u32 u32x2 __attribute__((ext_vector_type(2)));
typedef u64 u64x8 __attribute__((ext_vector_type(8)));
typedef float f32x2 __attribute__((ext_vector_type(2)));
typedef float f32x4 __attribute__((ext_vector_type(4)));
typedef float f32x16 __attribute__((ext_vector_type(16)));
typedef __bf16 bf16x8 __attribute__((ext_vector_type(8)));
typedef __bf16 bf16x2 __attribute__((ext_vector_type(2)));
typedef short s16x4 __attribute__((ext_vector_type(4)));
typedef __amdgpu_buffer_rsrc_t rsrc_t;
#define LDSP(T) __attribute__((address_space(3))) T*
#define DI __device__ __forceinline__

constexpr u32 WS_E0 = 0;
constexpr u32 WS_FL = 1024;
constexpr u32 WS_KVR = 16384;
constexpr u32 WS_KX = 256 * 1024;
constexpr u32 WS_P = 1024 * 1024;
constexpr u32 WS_OA = 3 * 1024 * 1024;
constexpr u32 WS_OB = 5 * 1024 * 1024;
constexpr u32 WS_BYTES = 7 * 1024 * 1024;
constexpr u32 slot(bool A, int c) { return A ? (c & 3) * 32768 : 16384 + ((6 - c) % 6) * 24576; }
constexpr u32 sbase(bool A, int c) { return A ? (c & 2) * 32768 : c == 1 || c == 2 ? slot(false, 2) : c == 3 || c == 4 ? slot(false, 4) : 0; }
constexpr u32 wofs(int ks) { return (6 - (ks >> 2)) * 8192 + (ks & 3) * 2048; }
constexpr u32 L_WV = 104 * 1024, L_MISC = 0, L_PHI = 0, L_VP = 32 * 1024, L_AX = 0;
constexpr u32 OOB = 0x80000000u, ONES = 0x3F803F80u;
constexpr int SPIN = 1 << 15;
constexpr int SC1 = 16;
#define KVL 0
#define ER 1
#define EK 0
#define BT 0
#define TB 0
#define TBC 0
#define RW 1
#define AW 0
#define BW 0
#define BP 0
#define NAP 3
#define EP 0
#define BOF 0
#define D1 0
#define D2 0
#define AT 1
#define PK 16
#define PC1 4
#define KC 14
#define EPV 0
#define AORD 0
#define W1X 1
#define VB0 2
#define PRIO 1
#define PPOLL 9
#define KPOLL 20
template <int N> struct ic { static constexpr int value = N; };
constexpr float LOG2E = 1.4426950408889634f;

DI void dma(u32 m0v, u32 voff, rsrc_t rs, u32 soff) {
    asm volatile("s_mov_b32 m0, %0\n\ts_nop 0\n\tbuffer_load_dwordx4 %1, %2, %3 offen lds" ::"s"(m0v), "v"(voff), "s"(rs), "s"(soff) : "memory");
}
DI void dmax(u32 m0v, u32 voff, rsrc_t rs, u32 soff, u64 mask) {
    asm volatile("s_mov_b32 m0, %0\n\ts_mov_b64 exec, %4\n\tbuffer_load_dwordx4 %1, %2, %3 offen lds\n\ts_mov_b64 exec, -1" ::"s"(m0v), "v"(voff), "s"(rs), "s"(soff), "s"(mask) : "memory");
}
DI u32 touch(u32 voff, rsrc_t rs) {
    u32 r; asm volatile("buffer_load_dword %0, %1, %2, 0 offen" : "=v"(r) : "v"(voff), "s"(rs) : "memory"); return r;
}
DI void drop(u32 r) { asm volatile("" ::"v"(r)); }
template <int N> DI void waitbar() { asm volatile("s_waitcnt vmcnt(%0) lgkmcnt(0)\n\ts_barrier" ::"i"(N) : "memory"); }
#define WAITBAR(N) asm volatile("s_waitcnt vmcnt(" #N ") lgkmcnt(0)\n\ts_barrier" ::: "memory")
#define VBAR(N) asm volatile("s_waitcnt vmcnt(" #N ")\n\ts_barrier" ::: "memory")
#define VM0() __builtin_amdgcn_s_waitcnt(0x0F70)
#define LBAR() asm volatile("s_waitcnt lgkmcnt(0)\n\ts_barrier" ::: "memory")
#define BAR() asm volatile("s_barrier" ::: "memory")
#define SCHED() __builtin_amdgcn_sched_barrier(0)
DI u32x4 r128(u32 a) { return *(LDSP(const u32x4))(size_t)a; }
DI void w128(u32 a, u32x4 v) { *(LDSP(u32x4))(size_t)a = v; }
DI void w64(u32 a, u32x2 v) { *(LDSP(u32x2))(size_t)a = v; }
DI u32x2 rtr(u32 a) { return __builtin_bit_cast(u32x2, __builtin_amdgcn_ds_read_tr16_b64_v4i16((LDSP(s16x4))(size_t)a)); }
DI u32x4 rtr2(u32 a, u32 b) { u32x2 x = rtr(a), y = rtr(b); return u32x4{x.x, x.y, y.x, y.y}; }
DI f32x16 mm32(u32x4 a, u32x4 b, f32x16 c) { return __builtin_amdgcn_mfma_f32_32x32x16_bf16(__builtin_bit_cast(bf16x8, a), __builtin_bit_cast(bf16x8, b), c, 0, 0, 0); }
DI f32x4 mm16(u32x4 a, u32x4 b, f32x4 c) { return __builtin_amdgcn_mfma_f32_16x16x32_bf16(__builtin_bit_cast(bf16x8, a), __builtin_bit_cast(bf16x8, b), c, 0, 0, 0); }
DI u32 pk(float lo, float hi) { f32x2 v = {lo, hi}; return __builtin_bit_cast(u32, __builtin_convertvector(v, bf16x2)); }
DI u32x4 pk8(const f32x16& a, int s) { return u32x4{pk(a[8 * s], a[8 * s + 1]), pk(a[8 * s + 2], a[8 * s + 3]), pk(a[8 * s + 4], a[8 * s + 5]), pk(a[8 * s + 6], a[8 * s + 7])}; }
DI float f_(u32 x) { return __builtin_bit_cast(float, x); }
DI u32 u_(float x) { return __builtin_bit_cast(u32, x); }
DI float max2(float a, float b) { return __builtin_elementwise_maximum(a, b); }
DI float max3(float a, float b, float c) { return max2(max2(a, b), c); }
DI float mul1(float a, float b) { float r; asm("v_mul_f32 %0, %1, %2" : "=v"(r) : "v"(a), "v"(b)); return r; }
DI u32 bfi(u32 m, u32 a, u32 b) { return (a & m) | (b & ~m); }
DI void pin(f32x16& x) { asm volatile("" : "+v"(x)); }
DI float xmax32(float x) { auto r = __builtin_amdgcn_permlane32_swap(u_(x), u_(x), false, false); return max2(f_(r[0]), f_(r[1])); }
DI float xadd32(float x) { auto r = __builtin_amdgcn_permlane32_swap(u_(x), u_(x), false, false); return f_(r[0]) + f_(r[1]); }
#define LAUX SC1
#define SAUX SC1
DI u32x4 gld(rsrc_t rs, u32 off) { return __builtin_amdgcn_raw_buffer_load_b128(rs, off, 0, LAUX); }
DI void gst(rsrc_t rs, u32 off, u32x4 v) { __builtin_amdgcn_raw_buffer_store_b128(v, rs, off, 0, SAUX); }
DI void gst64(rsrc_t rs, u32 off, u32x2 v) { __builtin_amdgcn_raw_buffer_store_b64(v, rs, off, 0, SC1); }
DI void gst32(rsrc_t rs, u32 off, u32 v) { __builtin_amdgcn_raw_buffer_store_b32(v, rs, off, 0, SAUX); }
DI u32 opq(u32 x) { asm("" : "+v"(x)); return x; }
DI u32 opv(u32 x) { asm volatile("" : "+v"(x)); return x; }
DI float sel(u32 m, float a, float b) { return f_((u_(a) & m) | (u_(b) & ~m)); }
template <int N> DI void pause() { if (N) asm volatile("s_sleep %0" ::"i"(N) : "memory"); }
DI void nap() { asm volatile("s_sleep 1" ::: "memory"); }
DI bool none(bool bad) { return __builtin_amdgcn_ballot_w64(bad) == 0; }
DI u32x4 max(u32x4 a, u32x4 b) { return __builtin_elementwise_max(a, b); }
DI bool unset(u32x4 v) { return max(max(v.x, v.y), max(v.z, v.w)) == ~0u; }
DI rsrc_t mk(const void* p, u32 bytes) { return __builtin_amdgcn_make_buffer_rsrc((void*)p, 0, bytes, 0x00020000); }

template <bool NORM, class H, class E>
DI void phi(const f32x16& p, float keep, H hook, E emit) {
    float mx = max2(p[0], p[1]), nm = max2(-p[0], -p[1]);
    auto mstep = [&](int i) { mx = max3(p[i], p[i + 1], mx); nm = max3(-p[i], -p[i + 1], nm); };
    mstep(2); hook(ic<0>{}); mstep(4); mstep(6); hook(ic<1>{}); mstep(8); mstep(10); hook(ic<2>{}); mstep(12); mstep(14); hook(ic<3>{});
    {
        auto r = __builtin_amdgcn_permlane32_swap(u_(mx), u_(nm), false, false);
        const float m = max2(f_(r[0]), f_(r[1])) * -LOG2E;
        auto q = __builtin_amdgcn_permlane32_swap(u_(m), u_(m), false, false);
        mx = f_(q[0]); nm = f_(q[1]);
    }
    hook(ic<4>{});
    float a[16], b[16];
    float sa = 0.f, sb = 0.f;
    auto step = [&](auto ii) {
        constexpr int i = 15 - decltype(ii)::value;
        a[i] = __builtin_amdgcn_exp2f(fmaf(p[i], LOG2E, mx)); b[i] = __builtin_amdgcn_exp2f(fmaf(p[i], -LOG2E, nm));
        if (NORM) {
            if constexpr (i == 13) { sa = a[15] + a[14]; sb = b[15] + b[14]; }
            else if constexpr (i == 10) { sa = fmaf(sa, keep, a[11]); sb = fmaf(sb, keep, b[11]); }
            else if constexpr (i < 13) { sa += a[i + 1]; sb += b[i + 1]; }
        }
        hook(ic<20 - i>{});
    };
    step(ic<0>{}); step(ic<1>{}); step(ic<2>{}); step(ic<3>{}); step(ic<4>{}); step(ic<5>{}); step(ic<6>{}); step(ic<7>{});
    step(ic<8>{}); step(ic<9>{}); step(ic<10>{}); step(ic<11>{}); step(ic<12>{}); step(ic<13>{}); step(ic<14>{}); step(ic<15>{});
    if (NORM) {
        sa += a[0]; sb += b[0];
        auto r = __builtin_amdgcn_permlane32_swap(u_(sa), u_(sb), false, false);
        const float m = __builtin_amdgcn_rcpf(f_(r[0]) + f_(r[1]));
        auto q = __builtin_amdgcn_permlane32_swap(u_(m), u_(m), false, false);
        sa = f_(q[0]); sb = f_(q[1]);
    }
    auto out = [&](auto ss) {
        constexpr int s = decltype(ss)::value;
        hook(ic<21 + s>{});
        float* e = (s & 2) ? b + 8 * (s & 1) : a + 8 * (s & 1);
        if (NORM) {
#pragma unroll
            for (int i = 0; i < 8; ++i) e[i] = mul1(e[i], (s & 2) ? sb : sa);
        }
        emit(ss, u32x4{pk(e[0], e[1]), pk(e[2], e[3]), pk(e[4], e[5]), pk(e[6], e[7])});
    };
    out(ic<0>{}); out(ic<1>{}); out(ic<2>{}); out(ic<3>{});
}

#define STAMP(i)
#define STAMPB(i)

struct Args { const void *x, *wa, *wb, *wphi, *gate; void* ws; const void *woa, *wob; void* out; const void* pad; };
template <bool isA>
DI void body(const Args& ar, const u32 lb) {
    STAMP(0);
    const u32 tid = threadIdx.x, l = tid & 63;
    const u32 w = __builtin_amdgcn_readfirstlane(tid >> 6);
    const u32 b = blockIdx.x, xcd = b & 7, bi = b >> 3;
    u64x8 ka; u64 kb;
    asm volatile("s_load_dwordx16 s[8:23], %2, 0x0\n\ts_load_dwordx2 s[24:25], %2, 0x40" : "=&{s[8:23]}"(ka), "=&{s[24:25]}"(kb) : "s"(__builtin_amdgcn_kernarg_segment_ptr()) : "memory");
    const u32 t = (xcd >> 1) * 4 + ((bi & 15) >> 2), j = (xcd & 1) * 4 + (bi & 3);
    const u32 rg = w & 3, hh = w >> 2, head = 2 * j + hh;
    const u32 tok = l & 31, h = l >> 5, p = l & 15, g = l >> 4;
    constexpr u32 L_WQ = isA ? 104 * 1024 : 48 * 1024, L_WOA = isA ? 48 * 1024 : 104 * 1024, L_WOB = isA ? 104 * 1024 : 0, L_XO = isA ? 0 : 56 * 1024;
    const u32 a16 = bi & 15, jb = (xcd & 1) * 3;
    const u32 un = t >> 1, cg = (t & 1) * 8 + w, gq = cg >> 2;
    constexpr u32 KV2 = 512;
    const u32 kvd = 16 * (un & 1) + ((4 * cg + (l & 3)) & 15);
    const u32 kvo = WS_KVR + ((2 * j + (un >> 2)) * 4 + ((un >> 1) & 1) * 2 + (gq >> 1)) * 1024 + (kvd + 32 * (gq & 1)) * 16 + g * 4;
    u32 vx0 = (t * 128 + w * 16 + (l >> 3)) * 896 + (((l & 7) ^ g) << 4);
    u32 vx1 = (vx0 + 8 * 896) ^ 64;
    const u32 wsrc = (l & 7) ^ ((g & 1) << 2);
    u32 vw = wsrc == 7 ? OOB : (w * 8 + (l >> 3)) * 2688 + j * 112 + wsrc * 16;
    const bool sp = w == 0 && a16 < 3;
    u32 o1 = sp ? (a16 < 2 ? (xcd & 1) * 12544 + a16 * 8192 : 0) + l * 128
                : a16 < 8 ? ((t & 12) * 128 + a16 * 64 + l) * 896 + w * 128
                          : (w * 64 + (a16 & 7) * 8 + (l >> 3)) * 2688 + (((l >> 2) & 1) * 7 + 7 + jb + (l & 3)) * 128;
    if (!(sp || (w < 7 && (w >= W1X || (a16 >= 8 && (l & 4)))))) o1 = OOB;
    if (TB && !sp) o1 = w < 7 && w >= W1X && l < 32 ? ((t & 12) * 128 + a16 * 32 + l) * 896 + w * 128 : OOB;
    u32 kvc = (l & 8) || w >= 4 ? OOB : kvo + (l & 4) * (KV2 / 4);
    if (isA) asm volatile("s_waitcnt lgkmcnt(0)" : "+{s[8:23]}"(ka), "+{s[24:25]}"(kb), "+v"(o1));
    else asm volatile("s_waitcnt lgkmcnt(0)" : "+{s[8:23]}"(ka), "+{s[24:25]}"(kb), "+v"(vx0), "+v"(vx1), "+v"(vw), "+v"(kvc));
    asm volatile("" : "+s"(kb));
    const void *gx = (const void*)ka[0], *gwa = (const void*)ka[1], *gwb = (const void*)ka[2], *gwphi = (const void*)ka[3], *ggate = (const void*)ka[4], *gwoa = (const void*)ka[6], *gwob = (const void*)ka[7];
    void *gws = (void*)ka[5], *gout = (void*)kb;
    u64 tg;
    u32x2 tgv = {0, 0};
    if (!EPV) asm volatile("s_load_dwordx2 s[90:91], %1, 0x0" : "=&{s[90:91]}"(tg) : "s"(gws) : "memory");
    const rsrc_t rx = mk(gx, 2048 * 896), rw = mk(isA ? gwa : gwb, 448 * 2688), rs = mk(gws, WS_BYTES);
    u32 jk0 = 0, jk1 = 0, jk2 = 0;
    auto t1 = [&]() {
        const rsrc_t rc = mk(gws, 0);
        jk1 = touch(w < 7 ? (w >= (AORD >= 2) && l < 32 ? (w * 64 + a16 * 4 + (l >> 3)) * 2688 + (((l >> 2) & 1) * 7 + 7 + jb + (l & 3)) * 128 : OOB) : a16 * 8192 + l * 128, w < 7 ? rw : rc);
    };
    auto t2 = [&]() { jk2 = touch(w < 2 && (w * 16 + (l >> 2)) < 28 ? (a16 * 28 + w * 16 + (l >> 2)) * 2688 + (jb + (l & 3)) * 128 : OOB, mk(gwb, 448 * 2688)); };
    if constexpr (isA) {
        const rsrc_t rwb = mk(gwb, 448 * 2688);
        jk0 = touch(o1, sp ? (a16 < 2 ? mk(gwphi, 16 * 1568) : mk(ggate, 896)) : TB || a16 < 8 ? rx : rwb);
        if (TB) jk2 = touch(w < 7 && w >= W1X && l < 16 ? (w * 64 + a16 * 4 + (l >> 2)) * 2688 + (7 + jb + (l & 3)) * 128 : OOB, rwb);
        BAR();
        if (AORD != 2) { t1(); BAR(); }
        if (AORD == 0 && !TB) { t2(); BAR(); }
    } else if (!KVL) gst32(rs, kvc, ~0u);

    auto chunk1 = [&](int c, int q) {
        const u32 sl = lb + slot(isA, c);
        if (q == 0) dma(sl + w * 2048, vx0, rx, c * 128);
        if (q == 1) dma(sl + w * 2048 + 1024, vx1, rx, c * 128);
        if (q == 2) dma(sl + 16384 + w * 1024, vw, rw, c * (64 * 2688) + 896);
        if (q == 3 && isA) dma(sl + 24576 + w * 1024, vw, rw, c * (64 * 2688) + 1792);
    };
    auto chunk = [&](int c) { chunk1(c, 0); chunk1(c, 1); chunk1(c, 2); chunk1(c, 3); };
    auto wv_dma = [&](int q) { dma(lb + L_WV + wofs(4 * q) + w * 1024, vw, rw, q * (64 * 2688) + 1792); };
    auto wq_dma = [&](int q) { dmax(lb + L_WQ + wofs(4 * q) + w * 1024, vw, rw, q * (64 * 2688), 0xF7F77F7FF7F77F7Full); };
    chunk(0);
    BAR();
    if (isA && AORD == 2) { t1(); BAR(); }
    STAMP(1);
    chunk(1);
    BAR();
    STAMP(14);
    chunk(2);
    if (CPF == 3 && isA) asm volatile(".Lfm_begin:");
    if (!isA && KVL) gst32(rs, kvc, ~0u);
    STAMP(13);
    u32x2 tag;
    auto mktag = [&]() { tg += 0x9E3779B97F4A7C15ull; tag = u32x2{(u32)tg, (u32)(tg >> 32)}; };
    if (!EPV) { asm volatile("s_waitcnt lgkmcnt(0)" : "+{s[90:91]}"(tg)); mktag(); }
    auto eload = [&]() { if (EPV) asm volatile("buffer_load_dwordx2 %0, off, %1, 0" : "=v"(tgv) : "s"(rs) : "memory"); };
    auto etag = [&]() { if (EPV) { asm volatile("" : "+v"(tgv)); tg = __builtin_amdgcn_readfirstlane(tgv.x) | ((u64)__builtin_amdgcn_readfirstlane(tgv.y) << 32); mktag(); } };
    u32 gatev = 0;
    auto misc = [&]() {
        if (!isA && !hh) dma(lb + L_MISC + w * 1024, (w & 1) ? (l < 34 ? l * 16 + 1024 : OOB) : l * 16, mk(gwphi, 16 * 1568), (2 * j + (w >> 1)) * 1568);
        else gatev = touch(tid * 4 - 1024, mk(ggate, 896));
    };
    auto gate_col = [&]() {
        asm volatile("" : "+v"(gatev));
        if (tid - 256 < 224) {
            const u32 r = 2 * tid - 512, ga = lb + L_WQ + (6 - (r >> 6)) * 8192 + (r & 63) * 128 + ((7 ^ ((r & 2) << 1)) << 4);
            w128(ga, u32x4{gatev & 0xFFFFu, 0, 0, 0}); w128(ga + 128, u32x4{gatev >> 16, 0, 0, 0});
        }
    };
    auto flag = [&]() { gst64(rs, tid == 0 ? WS_FL + b * 8 : OOB, tag); };

    const u32 xa = lb + (rg * 32 + tok) * 128 + ((h ^ ((tok >> 1) & 7)) << 4);
    const u32 wrow = lb + (8 * h + (p >> 2)) * 128, wcol = 32 * (g & 1) + 8 * (p & 3), wsw = ((p >> 3) & 1) << 6;
    const u32 wa = wrow + ((56 * hh + wcol) ^ wsw);
    u32x4 xf[28], wphi[2];
    f32x16 ak = {}, av = {};
    u32x4 wk[2][4], wv[4];
    u32 xb, wb;
    auto rd = [&](int c, int s) {
        const u32 sl = slot(isA, c) - sbase(isA, c);
        xf[4 * c + s] = r128((xb ^ (32 * s)) + sl);
        wk[isA ? 0 : c & 1][s] = rtr2(wb + sl + 16384 + s * 2048, wb + sl + 16384 + s * 2048 + 512);
        if (isA) wv[s] = rtr2(wb + sl + 24576 + s * 2048, wb + sl + 24576 + s * 2048 + 512);
    };
    auto mk_ = [&](int c, int s) { if (c >= 0) { ak = mm32(wk[isA ? 0 : c & 1][s], xf[4 * c + s], ak); pin(ak); } SCHED(); };
    auto mv_ = [&](int ks) { if (ks >= 0) { av = mm32(xf[ks], wv[ks & 3], av); pin(av); } SCHED(); };
    auto issue = [&](int c, int q) {
        if (c < 4) { chunk1(c + 3, q); if (!isA && c == 3 && q == 3) misc(); if (isA && AORD && c == 0 && q == 3) t2();
            if (!isA && TB && c == TBC && q == 3) jk2 = touch(l < 32 && w < 7 ? (a16 * 28 + w * 4 + ((l >> 2) & 3)) * 2688 + (jb + (l & 3) + (l < 16 ? 14 : 0)) * 128 : OOB, rw); }
        else if (isA) {
            const int n = 4 * (c - 4) + q;
            if (n == 0) misc(); else if (n < 8) wq_dma(n - 1);
            else if (n == 8) jk0 = touch(l < 25 ? (a16 * 196 + w * 25 + l) * 128 : OOB, mk(gwob, 448 * 896));
        } else if (q < 3) {
            const int n = 3 * (c - 4) + q;
            if (n < 7) wv_dma(n);
            else if (n == 7) jk0 = touch(l < 16 && w < 7 ? (a16 * 28 + w * 4 + (l >> 2)) * 2688 + (jb + (l & 3)) * 128 : OOB, mk(gwa, 448 * 2688));
            else jk1 = touch(l < 25 ? (a16 * 196 + w * 25 + l) * 128 : OOB, mk(gwoa, 448 * 896));
        }
        SCHED();
    };
    auto iter = [&](auto cc) {
        constexpr int c = decltype(cc)::value;
        constexpr int W[7] = {6 + KVL, 6 + KVL, 6 + KVL, 6, 7, 7, 6};
        if (c == 0 || sbase(isA, c) != sbase(isA, c - 1)) { xb = opq(xa + sbase(isA, c)); wb = opq(wa + sbase(isA, c)); }
        constexpr int WA[7] = {8 + (AORD == 2), 8 + !!AORD, 8 + !!AORD, 8 + !!AORD, 8, 8, 8};
        waitbar<isA ? WA[c] : W[c] + (TB && c > TBC && c <= TBC + 3)>();
        if (c == 0) drop(jk0);
        if (c == (AORD == 2)) drop(jk1);
        if (c == (isA ? (AORD ? 4 : 0) : TBC + 4)) drop(jk2);
        STAMP(16 + c);
        rd(c, 0); rd(c, 1);
        SCHED();
        if constexpr (isA) {
            mk_(c - 1, 2); issue(c, 0); mv_(4 * c - 2); issue(c, 1);
            mk_(c - 1, 3); issue(c, 2); mv_(4 * c - 1); issue(c, 3);
            rd(c, 2); rd(c, 3);
            SCHED();
            mk_(c, 0); mv_(4 * c); mk_(c, 1); mv_(4 * c + 1);
        } else {
            mk_(c - 1, 1);
            issue(c, 0);
            rd(c, 2); rd(c, 3);
            SCHED();
            mk_(c - 1, 2);
            issue(c, 1);
            mk_(c - 1, 3);
            issue(c, 2);
            if (c == 6) {
                const u32 fq = 4 * (g & 1) + (p & 3);
                const u32 pa = lb + L_MISC + hh * 2048 + (4 * h + (p >> 2)) * 56 + (fq == 7 ? 6 : fq) * 8;
                wphi[0] = rtr2(pa, pa + 8 * 56); wphi[1] = rtr2(pa + 16 * 56, pa + 24 * 56);
                gate_col();
            }
            SCHED();
            mk_(c, 0);
            issue(c, 3);
        }
    };
    iter(ic<0>{}); iter(ic<1>{}); iter(ic<2>{}); iter(ic<3>{}); iter(ic<4>{}); iter(ic<5>{}); iter(ic<6>{});
    if (!isA) mk_(6, 1);
    mk_(6, 2); mk_(6, 3);
    if (isA) { mv_(26); mv_(27); }
    const u32x4 kf0 = pk8(ak, 0), kf1 = pk8(ak, 1);
    STAMP(2);

    const u32 i2 = (bi + 28) & 31;
    const bool p2 = i2 < 28;
    const u32 u2 = xcd * 4 + i2 / 7, cb = i2 % 7;
    const u32 vwo = (rg * 8 + (l >> 3)) * 896 + cb * 128 + (((l & 7) ^ (g << 1)) << 4);
    const rsrc_t roa = mk(gwoa, p2 ? 448 * 896 : 0), rob = mk(gwob, p2 ? 448 * 896 : 0);
    auto wob_dma = [&]() {
#pragma unroll
        for (int n = 0; n < 7; ++n) dma(lb + L_WOB + n * 8192 + w * 1024, vwo, rob, (2 * n + hh) * (32 * 896));
    };
    auto stage2 = [&](auto early) {
        f32x16 aq = {};
        const u32 wq = opq(wrow + L_WQ + ((wcol == 56 ? 112 : 56 * hh + wcol) ^ wsw));
        u32x4 f[3];
        auto rq = [&](int ks) { if (ks < 28) f[ks % 3] = rtr2(wq + wofs(ks), wq + wofs(ks) + 512); };
        if (PRIO && !hh) __builtin_amdgcn_s_setprio(1);
        if (hh) {
#pragma unroll
            for (int n = 0; n < 14; ++n) dma(lb + L_WOA + n * 4096 + rg * 1024, vwo, roa, n * (32 * 896));
        }
        rq(0); rq(1);
#pragma unroll
        for (int ks = 0; ks < 28; ++ks) {
            rq(ks + 2);
            aq = mm32(f[ks % 3], xf[ks], aq);
            pin(aq);
            SCHED();
            if (ks == (isA ? KPOLL : PK)) early();
            SCHED();
        }
        return aq;
    };
    auto tagged = [&](u32x4& a, u32x4& c) {
        auto r = __builtin_amdgcn_permlane32_swap(c.z, c.w, false, false);
        c.z = r[0]; c.w = bfi(0xFFFF0000u, a.w, tag.y); a.w = bfi(0x0000FFFFu, a.w, tag.y);
    };
    const u32 ooff = ((t * 8 + rg * 2 + (tok >> 4)) * 16 + head) * 1024 + h * 256 + p * 16;
    auto publish_o = [&](const f32x16& o, u32 base) {
        u32x4 o0 = pk8(o, 0), o1 = pk8(o, 1);
        tagged(o0, o1);
        gst(rs, base + ooff, o0); gst(rs, base + ooff + 512, o1);
    };
    const u32 th = w & 1, kq = w >> 1;
    const u32 oo = ((u2 * 4 + th * 2) * 16 + kq * 4) * 1024 + l * 16;
    u32x4 of[8];
    auto load_o = [&](u32 base, u32 from) {
#pragma unroll
        for (u32 e = 0; e < 8; ++e) { of[e] = gld(rs, base + oo + (e >> 1) * 1024 + (e & 1) * 16384); SCHED(); }
    };
    const u32 pro = l < 8 ? ((u2 * 4 + th * 2) * 16 + kq * 4 + (l >> 1)) * 1024 + (l & 1) * 16384 + 1020 : OOB;
    auto probe = [&](u32 base) { return __builtin_amdgcn_raw_buffer_load_b32(rs, pro + base, 0, SC1); };
    int bud = SPIN;
    auto wait_o = [&](u32 base, u32 v) {
#pragma nounroll
        for (; bud > 0; --bud) {
            if (none(l < 8 && ((v ^ tag.y) & 0xFFFFu) != 0)) break;
            pause<NAP>();
            v = probe(base);
        }
    };
    u32 wr0[4], wr1[4];
    auto wadr = [&]() {
        const u32 i = p >> 2, g1 = g & 1, r0 = 16 * (g >> 1) + 4 * g1 + i;
        const u32 mi = (u32)((int)(i - 2) >> 31), mg = (u32)((int)(g - 2) >> 31);
        const u32 r1 = bfi(mg, r0 + 8, bfi(mi, 24 + 2 * g1 + i, 8 + 4 * g1 + i));
        const u32 c = lb + L_WOA + 112 * 128 * kq + (p & 1) * 8;
        const u32 wb0 = c + r0 * 128 + ((((p & 3) >> 1) ^ (r0 & 6)) << 4), wb1 = c + r1 * 128 + ((((p & 3) >> 1) ^ (r1 & 6)) << 4);
#pragma unroll
        for (int x = 0; x < 4; ++x) { wr0[x] = opv(wb0 ^ (x << 5)); wr1[x] = opv(wb1 ^ (x << 5)); }
        SCHED();
    };
    auto wfrags = [&](u32x4 (&wf)[4][4], u32 img) {
#pragma unroll
        for (int e = 0; e < 4; ++e)
#pragma unroll
            for (int nt = 0; nt < 4; ++nt) { const int x = nt ^ (2 * (e & 1)); wf[e][nt] = rtr2(wr0[x] + e * 3584, wr1[x] + e * 3584); }
        if (img == L_WOA) {
#pragma unroll
            for (int x = 0; x < 4; ++x) { wr0[x] = opv(wr0[x] + (L_WOB - L_WOA)); wr1[x] = opv(wr1[x] + (L_WOB - L_WOA)); }
            SCHED();
        }
    };

    auto passes = [&](f32x4 (&cc)[2][4], const u32x4 (&wf)[4][4], u32 base) {
        const u32 tm = g < 2 ? 0xFFFF0000u : 0x0000FFFFu, ts = tag.y & tm;
        f32x4 t_[2][4];
#pragma nounroll
        for (;; --bud) {
            u32 acc = 0;
#pragma unroll
            for (u32 e = 0; e < 8; ++e) {
                u32x4 f = of[e];
                f.w ^= ts; acc |= f.w;
#pragma unroll
                for (int nt = 0; nt < 4; ++nt) t_[e & 1][nt] = mm16(wf[e >> 1][nt], f, e < 2 ? cc[e & 1][nt] : t_[e & 1][nt]);
                SCHED();
            }
            if (none((acc & tm) != 0) | (bud <= 0)) break;
            if (RW) wait_o(base, probe(base)); else asm volatile("" ::: "memory");
            load_o(base, 0);
        }
#pragma unroll
        for (int i = 0; i < 8; ++i) cc[i >> 2][i & 3] = t_[i >> 2][i & 3];
    };
    auto leave = [&]() {
        if (!isA) VM0();
    };
    auto finish = [&](const f32x4 (&cc)[2][4]) {
        STAMP(11);
        const u32 xo = lb + L_XO + kq * 16384 + (th * 32 + p) * 256 + ((g ^ (p & 7)) << 4);
#pragma unroll
        for (int nt = 0; nt < 4; ++nt) { w128(xo ^ (nt << 6), __builtin_bit_cast(u32x4, cc[0][nt])); w128((xo ^ (nt << 6)) + 4096, __builtin_bit_cast(u32x4, cc[1][nt])); }
        LBAR();
        const u32 tk = tid >> 3, c8 = tid & 7;
        const u32 xr = lb + L_XO + tk * 256 + (((2 * c8) ^ (tk & 7)) << 4);
        auto rsum = [&](u32 a) { auto f = [&](u32 o) { return __builtin_bit_cast(f32x4, r128(a + o)); }; return (f(0) + f(16384)) + (f(32768) + f(49152)); };
        const f32x4 r0 = rsum(xr), r1 = rsum(xr ^ 16);
        gst(mk(gout, 2048 * 896), (u2 * 64 + tk) * 896 + cb * 128 + c8 * 16, u32x4{pk(r0[0], r0[1]), pk(r0[2], r0[3]), pk(r1[0], r1[1]), pk(r1[2], r1[3])});
        STAMP(12);
    };

    if constexpr (isA) {
        const u32 kxo = WS_KX + (t * 16 + head) * 3072 + l * 16;
        const u32x4 vf0 = pk8(av, 0), vf1 = pk8(av, 1);
        const u32 mlo = h ? 0u : 0x0000FFFFu, mhi = h ? 0xFFFF0000u : 0u;
        asm volatile("s_waitcnt vmcnt(1)" ::: "memory");
        gate_col();
        if (rg < 3) {
            const u32 ax = lb + L_AX + w * 3072 + l * 16;
            w128(ax, kf0); w128(ax + 1024, kf1); w128(ax + 2048, vf1);
        } else {
            u32x4 a = kf0, c = kf1;
            tagged(a, c); gst(rs, kxo, a); gst(rs, kxo + 1024, c);
            auto r = __builtin_amdgcn_permlane32_swap(vf1.w, vf1.x, false, false);
            gst(rs, kxo + 2048, u32x4{bfi(mlo, tag.y, r[1]), vf1.y, vf1.z, bfi(mhi, tag.y, vf1.w)});
        }
        if (w == 0) flag();
        LBAR();
        STAMP(3);
        u32x4 kp0, kp1, vp;
        const bool kx = rg == 0 && t > 0;
        const u32 kpo = kxo - 16 * 3072;
        const rsrc_t rk = mk(gws, kx ? WS_BYTES : 0);
        const f32x16 aq = stage2([&]() { kp0 = gld(rk, kpo); kp1 = gld(rk, kpo + 1024); vp = gld(rk, kpo + 2048); });
        STAMP(4);
        if (AW == 1 || AW == 2) { VBAR(3); drop(jk0); }
        if (kx) {
#pragma nounroll
            for (int it = 0;; ++it) {
                if (none(((bfi(0xFFFF0000u, kp0.w, kp1.w) ^ tag.y) | ((bfi(mlo, vp.x, vp.w) ^ tag.y) & (mlo | mhi))) != 0) | (it == SPIN)) break;
                asm volatile("" ::: "memory");
                kp0 = gld(rs, kpo); kp1 = gld(rs, kpo + 1024); vp = gld(rs, kpo + 2048);
            }
            VM0();
            kp0.w = bfi(0x0000FFFFu, kp0.w, kp1.w);
            kp1.w = __builtin_amdgcn_permlane32_swap(kp1.z, kp1.z, false, false)[1];
            vp.x &= ~mlo;
            vp.w = bfi(mhi, __builtin_amdgcn_permlane32_swap(vp.x, vp.x, false, false)[0], vp.w);
        } else {
            const u32 ax = lb + L_AX + (rg ? w - 1 : w) * 3072 + l * 16;
            kp0 = r128(ax); kp1 = r128(ax + 1024); vp = r128(ax + 2048);
        }
        STAMP(5);
        const float ge = 1.f + __builtin_amdgcn_exp2f(aq[12] * -LOG2E);
        u32x4 qf0 = pk8(aq, 0), qf1 = pk8(aq, 1);
        if (h) { qf1.z = 0; qf1.w = 0; }
        f32x16 so = mm32(kf0, qf0, f32x16{}); so = mm32(kf1, qf1, so);
        f32x16 sp = mm32(kp0, qf0, f32x16{}); sp = mm32(kp1, qf1, sp);
        if (AW == 1 || AW == 2) { SCHED(); wob_dma(); SCHED(); }
        const u32 om = opq(0u - (tok & 1));
        const u32 base = (tok & ~1u) - 4 * h, basep = (rg > 0 || t > 0) ? base + 32 : 1000u;
        float sv[12];
        const float NEG = -1e30f;
#pragma unroll
        for (int i = 0; i < 8; ++i) { const float v = sel(om, so[2 * i + 1], so[2 * i]); sv[i] = (base - (8 * (i >> 1) + 2 * (i & 1))) <= 14u ? v : NEG; }
#pragma unroll
        for (int i = 4; i < 8; ++i) { const float v = sel(om, sp[2 * i + 1], sp[2 * i]); sv[4 + i] = (basep - (8 * (i >> 1) + 2 * (i & 1))) <= 14u ? v : NEG; }
        float mx = max2(sv[0], sv[1]);
#pragma unroll
        for (int i = 2; i < 12; i += 2) mx = max3(sv[i], sv[i + 1], mx);
        const float sc = 0.18898223650461362f * LOG2E;
        mx = xmax32(mx) * -sc;
        float sum = 0.f;
        u32 pd[12];
        const u32 pm = om ^ 0x0000FFFFu;
#pragma unroll
        for (int i = 0; i < 12; ++i) { const float e = __builtin_amdgcn_exp2f(fmaf(sv[i], sc, mx)); sum += e; pd[i] = pk(e, e) & pm; }
        f32x16 o = mm32(vf0, u32x4{pd[0], pd[1], pd[2], pd[3]}, f32x16{});
        o = mm32(vf1, u32x4{pd[4], pd[5], pd[6], pd[7]}, o);
        o = mm32(vp, u32x4{pd[8], pd[9], pd[10], pd[11]}, o);
        const u32 cf = u_(__builtin_amdgcn_rcpf(xadd32(sum) * ge));
        publish_o(o * f_(__builtin_amdgcn_permlane32_swap(cf, cf, false, false)[1]), WS_OA);
        STAMP(8);
        u32x4 wfa[4][4], wfb[4][4];
        f32x4 cc[2][4] = {};
        u32 pv = 0;
        wadr();
        if (AW == 1 || AW == 2) {
            pv = probe(WS_OA);
            wfrags(wfa, L_WOA);
            if (AW == 2) { wait_o(WS_OA, pv); load_o(WS_OA, 0); LBAR(); } else WAITBAR(3);
            wfrags(wfb, L_WOB);
            SCHED();
            if (AW == 1) { wait_o(WS_OA, pv); load_o(WS_OA, 0); }
        } else {
            VBAR(2);
            drop(jk0);
            wob_dma();
            load_o(WS_OA, 0);
            wfrags(wfa, L_WOA);
            if (AW == 0) { WAITBAR(8); wfrags(wfb, L_WOB); }
        }
        if (EP == 1) pv = probe(WS_OB);
        if (PRIO) __builtin_amdgcn_s_setprio(0);
        STAMP(9);
        passes(cc, wfa, WS_OA);
        STAMP(10);
        if (EP == 2) {
#pragma nounroll
            for (int it = 0; it < SPIN; ++it) {
                const u32 v = __builtin_amdgcn_raw_buffer_load_b32(rs, l < 4 ? WS_KVR + (kq * 4 + l) * 4096 : OOB, 0, SC1);
                if (none(v == ~0u)) break;
                nap();
            }
            pause<D1>();
        } else {
            if (!EP) pv = probe(WS_OB);
            if (AW == 3) { BAR(); wfrags(wfb, L_WOB); }
            wait_o(WS_OB, pv);
        }
        load_o(WS_OB, 0);
        if (AW == 4) { BAR(); wfrags(wfb, L_WOB); }
        passes(cc, wfb, WS_OB);
        finish(cc);
        leave();
    } else {
        const float keep = h ? 0.f : 1.f;
        {
            f32x16 pp = mm32(wphi[0], kf0, f32x16{});
            pp = mm32(wphi[1], kf1, pp);
            pin(pp);
            STAMP(23);
            const u32 wvb = opq(wa + L_WV);
            auto rdv = [&](int ks) { if (ks < 28) wv[ks & 3] = rtr2(wvb + wofs(ks), wvb + wofs(ks) + 512); };
            const u32 F = (((tok >> 1) & 3) << 2) | (((tok >> 3) & 1) << 1) | (tok & 1);
            const u32 pw = lb + L_PHI + w * 4096 + tok * 128 + ((h ^ F) << 3);
            SCHED();
            phi<true>(pp, keep, [&](auto nn) {
                constexpr int n = decltype(nn)::value;
                constexpr int k0 = n <= VB0 ? 0 : (n - VB0 - 1) * 28 / (24 - VB0), k1 = n <= VB0 ? 0 : (n - VB0) * 28 / (24 - VB0);
                SCHED();
                if (n == VB0) { STAMPB(1); VBAR(6); STAMPB(14); rdv(0); rdv(1); }
#pragma unroll
                for (int k = k0; k < k1; ++k) {
                    if (k == 10) { STAMPB(13); VBAR(2); STAMPB(16); }
                    if (k == 20) STAMPB(17);
                    rdv(k + 2); mv_(k);
                    if (k >= 10 && k < 24 && !(k & 1)) wq_dma((k - 10) / 2);
                }
                SCHED();
            }, [&](auto ss, u32x4 v) {
                constexpr u32 s = decltype(ss)::value, cu = 8 * (s >> 1) + 2 * (s & 1);
                w64(pw ^ (cu << 3), u32x2{v.x, v.y});
                w64(pw ^ ((cu + 4) << 3), u32x2{v.z, v.w});
            });
            STAMPB(18);
            u32x4 vf0 = pk8(av, 0), vf1 = pk8(av, 1);
            if (tok == 28) { vf0 = u32x4{ONES, ONES, ONES, ONES}; vf1 = vf0; }
            w128(lb + L_VP + w * 2048 + l * 16, vf0); w128(lb + L_VP + w * 2048 + 1024 + l * 16, vf1);
        }
        STAMPB(19);
        LBAR();
        STAMP(3);
        {
            const u32 sign = rg >> 1, dt = rg & 1;
            const u32 F = ((2 * (g & 1) + (p >> 3)) << 2) | ((p >> 2) & 1);
            const u32 pr = lb + L_PHI + hh * 16384 + (16 * (g >> 1) + 4 * (g & 1) + (p >> 2)) * 128 + (((8 * sign + (p & 3)) ^ F) << 3);
            const u32 vr = lb + L_VP + hh * 8192 + g * 512 + (16 * dt + p) * 16;
            f32x4 c0 = {}, c1 = {};
#pragma unroll
            for (int r = 0; r < 4; ++r) {
                const u32x4 vv = r128(vr + r * 2048);
                c0 = mm16(rtr2(pr + r * 4096, (pr ^ 16) + r * 4096 + 1024), vv, c0);
                c1 = mm16(rtr2((pr ^ 32) + r * 4096, (pr ^ 48) + r * 4096 + 1024), vv, c1);
            }
            const u32 po = WS_P + (j * 8 + w) * 32768 + t * 1024 + l * 16;
#define TG(x, tt) bfi(0xFFFFF000u, u_(x), tt)
            gst(rs, po, u32x4{TG(c0[0], tag.x), TG(c0[1], tag.x >> 12), TG(c0[2], tag.y), TG(c0[3], tag.y >> 12)});
            gst(rs, po + 16384, u32x4{TG(c1[0], tag.x), TG(c1[1], tag.x >> 12), TG(c1[2], tag.y), TG(c1[3], tag.y >> 12)});
        }
        flag();
        STAMPB(20);
        WAITBAR(3);
        drop(jk0); drop(jk1);
        STAMP(4);
        const u32 po = WS_P + (j * 8 + un) * 32768 + (l >> 2) * 1024 + (4 * cg + (l & 3)) * 16;
        u32x4 pa[4], kv[4];
        auto ldp = [&]() { pa[0] = gld(rs, po); pa[1] = gld(rs, po + 16384); pa[2] = gld(rs, po + 256); pa[3] = gld(rs, po + 16384 + 256); };
        auto pok = [&]() {
            const u32x4 d = (pa[0] ^ pa[1]) | (pa[0] ^ pa[2]) | (pa[0] ^ pa[3]) | (pa[0] ^ u32x4{tag.x, tag.x >> 12, tag.y, tag.y >> 12});
            return none(((d.x | d.y | d.z | d.w) & 0xFFFu) != 0);
        };
        const u32 fla = lb + L_WQ + 8192 + 120;
        auto reduce = [&]() {
#pragma unroll
            for (int z = 0; z < 2; ++z) {
                const bool pad = z && (t & 1);
                const u32 mb = pad ? 0u : 0xFFFFF000u, cbit = pad ? 0u : 0x800u;
                float uu[4];
#pragma unroll
                for (int i = 0; i < 4; ++i) {
                    auto r = __builtin_amdgcn_permlane32_swap((pa[2 * z][i] & 0xFFFFF000u) | 0x800u, (pa[2 * z + 1][i] & mb) | cbit, false, false);
                    uu[i] = f_(r[0]) + f_(r[1]);
                }
                float ww[2];
#pragma unroll
                for (int i = 0; i < 2; ++i) {
                    auto r = __builtin_amdgcn_permlane16_swap(u_(uu[i]), u_(uu[i + 2]), false, false);
                    float y = f_(r[0]) + f_(r[1]);
                    y += f_(__builtin_amdgcn_update_dpp(0, u_(y), 0x128, 0xf, 0xf, false));
                    y += f_(__builtin_amdgcn_update_dpp(0, u_(y), 0x124, 0xf, 0xf, false));
                    ww[i] = y;
                }
                gst32(rs, (l & 12) ? OOB : kvo + z * KV2, kvd == 29 ? (pad && h ? 0u : ONES) : pk(ww[0], ww[1]));
            }
            if (EK) *(volatile LDSP(u32))(size_t)fla = 1u;
        };
        const u32 ko = WS_KVR + head * 4096 + l * 16;
        auto ldk2 = [&](rsrc_t r) { kv[0] = gld(r, ko); kv[1] = gld(r, ko + 1024); kv[2] = gld(r, ko + 2048); kv[3] = gld(r, ko + 3072); };
        auto ldk = [&]() { ldk2(rs); };
        auto kok = [&]() { return none(unset(max(max(kv[0], kv[1]), max(kv[2], kv[3])))); };
        const f32x16 aq = stage2([&]() { if (ER && !hh) ldp(); });
        STAMP(5);
        bool done = hh, kearly = false;
        u32x4 pq[4];
        float ge = 1.f + __builtin_amdgcn_exp2f(aq[12] * LOG2E);
        {
            f32x16 pp = mm32(wphi[0], pk8(aq, 0), f32x16{});
            pp = mm32(wphi[1], pk8(aq, 1), pp);
            phi<false>(pp, keep, [&](auto nn) {
                constexpr int n = decltype(nn)::value;
                if (ER && n == PC1) { SCHED(); if (!hh) { done = pok(); if (done) reduce(); else if (PC1 >= PPOLL) ldp(); } SCHED(); }
                if (n == PPOLL && (!ER || PC1 < PPOLL)) { SCHED(); if (!done) ldp(); SCHED(); }
                if (EK && n == KC) { SCHED(); kearly = __builtin_amdgcn_readfirstlane(*(volatile LDSP(u32))(size_t)fla) != 0 && (EK == 1 || hh); ldk2(mk(gws, kearly ? WS_BYTES : 0)); SCHED(); }
            }, [&](auto ss, u32x4 v) { pq[decltype(ss)::value] = v; });
            asm volatile("" : "+v"(pq[0]), "+v"(pq[1]), "+v"(pq[2]), "+v"(pq[3]), "+v"(ge));
        }
        STAMP(15);
        if (!done) {
#pragma nounroll
            for (int it = 0;; ++it) {
                if (pok() | (it == SPIN)) break;
                asm volatile("" ::: "memory");
                ldp();
            }
            if (KO) VM0();
            reduce();
        }
        auto ob = [&]() {
            STAMP(7);
            f32x16 op = mm32(kv[0], pq[0], f32x16{}), on = mm32(kv[2], pq[2], f32x16{});
            op = mm32(kv[1], pq[1], op); on = mm32(kv[3], pq[3], on);
            const float rd_ = __builtin_amdgcn_rcpf(fmaf(op[12], on[13], fmaf(on[12], op[13], 1e-6f * op[13] * on[13])) * ge);
            auto r = __builtin_amdgcn_permlane32_swap(u_(on[13] * rd_), u_(op[13] * rd_), false, false);
            auto r2 = __builtin_amdgcn_permlane32_swap(r[1], r[1], false, false);
            publish_o(op * f_(r2[0]) + on * f_(r2[1]), WS_OB);
        };
        wadr();
        if (hh) VM0();
        STAMP(6);
        if (CPF == 4) asm volatile(".Lfm_begin:");
        const bool pub = EK && kearly && kok();
        if (pub) ob();
        BAR();
        if (!BT && KO < 2) wob_dma();
        u32x4 wfa[4][4], wfb[4][4];
        if (!pub) ldk();
        wfrags(wfa, L_WOA);
        if (KO == 2) wob_dma();
        if (!pub) {
#pragma nounroll
            for (int it = 0;; ++it) {
                if (kok() | (it == SPIN)) break;
                asm volatile("" ::: "memory");
                ldk();
            }
            if (KO) asm volatile("" :: "v"(pa[0]), "v"(pa[1]), "v"(pa[2]), "v"(pa[3]));
            ob();
        } else VM0();
        STAMP(8);
        if (CPF == 2) asm volatile(".Lfm_begin:");
        if (p2) {
            if (KO >= 3) wob_dma();
            load_o(WS_OA, 0);
            if (KO == 4) WAITBAR(8); else if (KO < 3) LBAR();
            if (!BW && KO != 3) wfrags(wfb, L_WOB);
            f32x4 cc[2][4] = {};
            if (PRIO) __builtin_amdgcn_s_setprio(0);
            SCHED();
            STAMP(9);
            passes(cc, wfa, WS_OA);
            STAMP(10);
            u32 pv = 0;
            if (BP) pv = probe(WS_OB);
            if (BW == 2) { wfrags(wfb, L_WOB); SCHED(); }
            if (BP) wait_o(WS_OB, pv);
            load_o(WS_OB, 0);
            if (KO == 3) { WAITBAR(8); wfrags(wfb, L_WOB); }
            if (BW == 1) wfrags(wfb, L_WOB);
            passes(cc, wfb, WS_OB);
            finish(cc);
        } else if (b == 31 && w == 0) {
#pragma nounroll
            for (int it = 0; it < SPIN; ++it) {
                const u32x4 tg = {tag.x, tag.y, tag.x, tag.y};
                const u32x4 v = (gld(rs, WS_FL + l * 16) ^ tg) | (gld(rs, WS_FL + 1024 + l * 16) ^ tg);
                if (none((v.x | v.y | v.z | v.w) != 0)) break;
                nap();
            }
            gst64(rs, l == 0 ? WS_E0 : OOB, tag);
        }
        leave();
    }

}

__global__ __launch_bounds__(512, 2) void fused(const Args ar) {
    __shared__ __attribute__((aligned(1024))) char smem[163840];
    const u32 lb = (u32)(size_t)(LDSP(char))smem;
    if (blockIdx.x >= 128) body<true>(ar, lb); else body<false>(ar, lb);
}

extern "C" void kernel_launch(void* const* d_in, const int* in_sizes, int n_in, void* d_out, int out_size, void* d_ws, size_t ws_size, hipStream_t stream) {
    (void)in_sizes; (void)n_in; (void)out_size; (void)ws_size;
    Args a;
    a.x = d_in[0]; a.wa = d_in[1]; a.wb = d_in[4]; a.wphi = d_in[5]; a.gate = d_in[7]; a.ws = d_ws; a.woa = d_in[2]; a.wob = d_in[6]; a.out = d_out; a.pad = nullptr;
    hipLaunchKernelGGL(fused, dim3(256), dim3(512), 0, stream, a);
}
```
